```python
import torch

def _softmax(x, axis=-1):
    return torch.softmax(x, dim=axis)
def _gelu(x):
    return torch.nn.functional.gelu(x, approximate="tanh")
def _silu(x):
    return torch.nn.functional.silu(x)
def _sigmoid(x):
    return torch.sigmoid(x)
def _layernorm(x, axis=-1, eps=1e-5):
    m = x.mean(dim=axis, keepdim=True)
    v = x.var(dim=axis, keepdim=True, unbiased=False)
    return (x - m) / torch.sqrt(v + eps)
def _scatter_add(tgt, idx, src, axis=0):
    return tgt.clone().index_add_(axis, idx, src)
def _segment_sum(src, seg, n):
    out = torch.zeros((n,) + src.shape[1:], dtype=src.dtype)
    return out.index_add_(0, seg, src)
def _pad(x, pw):
    flat = tuple(v for pair in reversed(pw) for v in pair)
    return torch.nn.functional.pad(x, flat)

kernel_name = 'concat_heads__gqa_block_cached__mha_block_cached__near_ridge'

def setup_inputs():

    _w = (lambda a: (lambda t: t.to(torch.bfloat16) if t.is_floating_point() else t)(torch.as_tensor(a)))
    def _A_setup():
        B, Sq, D = 16, 48, 576
        Hq, Hkv, Sk, Dh = 32, 16, 48, 128
        assert Hq % Hkv == 0, (Hq, Hkv)
        return {
            "x": _w((lambda *s: torch.randn(*s, dtype=torch.bfloat16) * 0.8)(B, Sq, D)),
            "k_cache": _w((lambda *s: torch.randn(*s, dtype=torch.bfloat16) * 0.8)(B, Hkv, Sk, Dh)),
            "v_cache": _w((lambda *s: torch.randn(*s, dtype=torch.bfloat16) * 0.05)(B, Hkv, Sk, Dh)),
            "wq": _w((lambda *s: torch.randn(*s, dtype=torch.bfloat16) * 0.8)(D, Hq * Dh) * (D ** -0.5)),
            "wo": _w((lambda *s: torch.randn(*s, dtype=torch.bfloat16) * 0.05)(Hq * Dh, D) * ((Hq * Dh) ** -0.5)),
        }
    _A_in = _A_setup()

    def _B_setup():
        B, Sq, D = 16, 48, 576
        H, Sk, Dh = 32, 48, 128
        return {
            "x": _w((lambda *s: torch.randn(*s, dtype=torch.bfloat16) * 0.8)(B, Sq, D)),
            "k_cache": _w((lambda *s: torch.randn(*s, dtype=torch.bfloat16) * 0.8)(B, H, Sk, Dh)),
            "v_cache": _w((lambda *s: torch.randn(*s, dtype=torch.bfloat16) * 0.05)(B, H, Sk, Dh)),
            "wq": _w((lambda *s: torch.randn(*s, dtype=torch.bfloat16) * 0.8)(D, H * Dh) * (D ** -0.5)),
            "wo": _w((lambda *s: torch.randn(*s, dtype=torch.bfloat16) * 0.05)(H * Dh, D) * ((H * Dh) ** -0.5)),
        }
    _B_in = _B_setup()

    return {
        "A_x": _A_in["x"],
        "A_k_cache": _A_in["k_cache"],
        "A_v_cache": _A_in["v_cache"],
        "A_wq": _A_in["wq"],
        "A_wo": _A_in["wo"],
        "B_x": _B_in["x"],
        "B_k_cache": _B_in["k_cache"],
        "B_v_cache": _B_in["v_cache"],
        "B_wq": _B_in["wq"],
        "B_wo": _B_in["wo"],
    }

def reference(A_x, A_k_cache, A_v_cache, A_wq, A_wo, B_x, B_k_cache, B_v_cache, B_wq, B_wo):
    def _A_ref(x, k_cache, v_cache, wq, wo):
        B, Sq, D = x.shape
        Hq, Dh = 32, 128
        Hkv = k_cache.shape[1]
        rep = Hq // Hkv
        q = (lambda x, *d: x.permute(*d))((x @ wq).reshape(B, Sq, Hq, Dh), 0, 2, 1, 3)
        k = (lambda x, n, axis: torch.repeat_interleave(x, n, dim=axis))(k_cache, rep, axis=1)
        v = (lambda x, n, axis: torch.repeat_interleave(x, n, dim=axis))(v_cache, rep, axis=1)
        scores = torch.einsum("bhqd,bhkd->bhqk", q, k) * (Dh ** -0.5)
        p = _softmax(scores, axis=-1)
        o = torch.einsum("bhqk,bhkd->bhqd", p, v)
        return (lambda x, *d: x.permute(*d))(o, 0, 2, 1, 3).reshape(B, Sq, Hq * Dh) @ wo

    def _B_ref(x, k_cache, v_cache, wq, wo):
        B, Sq, D = x.shape
        H, Dh = 32, 128
        q = (lambda x, *d: x.permute(*d))((x @ wq).reshape(B, Sq, H, Dh), 0, 2, 1, 3)
        scores = torch.einsum("bhqd,bhkd->bhqk", q, k_cache) * (Dh ** -0.5)
        p = _softmax(scores, axis=-1)
        o = torch.einsum("bhqk,bhkd->bhqd", p, v_cache)
        return (lambda x, *d: x.permute(*d))(o, 0, 2, 1, 3).reshape(B, Sq, H * Dh) @ wo

    a = _A_ref(A_x, A_k_cache, A_v_cache, A_wq, A_wo)
    b = _B_ref(B_x, B_k_cache, B_v_cache, B_wq, B_wo)
    return torch.concatenate([a, b], axis=-1)
```

```cpp
#include <hip/hip_runtime.h>
#include <hip/hip_bf16.h>
#include <stdint.h>
typedef unsigned short u16;
__device__ __forceinline__ float bf2f(u16 x){ return __uint_as_float(((unsigned)x)<<16); }
__device__ __forceinline__ u16 f2bf(float f){ unsigned u=__float_as_uint(f); u += 0x7fff + ((u>>16)&1); return (u16)(u>>16); }

__global__ void k_attn(const u16* xA,const u16* kA,const u16* vA,const u16* wqA,
                       const u16* xB,const u16* kB,const u16* vB,const u16* wqB, u16* o){
  int id=blockIdx.x; int br=id>>9; int b=(id>>5)&15; int h=id&31;
  const u16* x=br?xB:xA; const u16* wq=br?wqB:wqA;
  const u16* kc= br? kB+((size_t)(b*32+h))*48*128 : kA+((size_t)(b*16+h/2))*48*128;
  const u16* vc= br? vB+((size_t)(b*32+h))*48*128 : vA+((size_t)(b*16+h/2))*48*128;
  __shared__ float q[48*128]; __shared__ float s[48*48];
  int t=threadIdx.x;
  for(int i=t;i<48*128;i+=256){ int r=i>>7,d=i&127; float a=0; for(int k=0;k<576;k++) a+=bf2f(x[(b*48+r)*576+k])*bf2f(wq[(size_t)k*4096+h*128+d]); q[i]=bf2f(f2bf(a)); }
  __syncthreads();
  for(int i=t;i<48*48;i+=256){ int r=i/48,c=i%48; float a=0; for(int d=0;d<128;d++) a+=q[r*128+d]*bf2f(kc[c*128+d]); s[i]=a*0.08838834764831845f; }
  __syncthreads();
  if(t<48){ float m=-1e30f; for(int c=0;c<48;c++) m=fmaxf(m,s[t*48+c]); float sum=0; for(int c=0;c<48;c++){ float e=__expf(s[t*48+c]-m); s[t*48+c]=e; sum+=e;} for(int c=0;c<48;c++) s[t*48+c]=bf2f(f2bf(s[t*48+c]/sum)); }
  __syncthreads();
  for(int i=t;i<48*128;i+=256){ int r=i>>7,d=i&127; float a=0; for(int c=0;c<48;c++) a+=s[r*48+c]*bf2f(vc[c*128+d]); o[(size_t)br*768*4096+(size_t)(b*48+r)*4096+h*128+d]=f2bf(a);}
}
__global__ void k_out(const u16* o,const u16* woA,const u16* woB,u16* out){
  int row=blockIdx.x; int br=blockIdx.y; const u16* wo=br?woB:woA;
  __shared__ float orow[4096];
  for(int i=threadIdx.x;i<4096;i+=blockDim.x) orow[i]=bf2f(o[(size_t)br*768*4096+(size_t)row*4096+i]);
  __syncthreads();
  int n=threadIdx.x; if(n<576){ float a=0; for(int k=0;k<4096;k++) a+=orow[k]*bf2f(wo[(size_t)k*576+n]); out[row*1152+br*576+n]=f2bf(a);}
}
extern "C" void kernel_launch(void* const* d_in, const int* in_sizes, int n_in,
                              void* d_out, int out_size, void* d_ws, size_t ws_size,
                              hipStream_t stream) {
  const u16* const* in=(const u16* const*)d_in;
  k_attn<<<1024,256,0,stream>>>(in[0],in[1],in[2],in[3],in[5],in[6],in[7],in[8],(u16*)d_ws);
  k_out<<<dim3(768,2),576,0,stream>>>((u16*)d_ws,in[4],in[9],(u16*)d_out);
}
```

```cpp
#include <hip/hip_runtime.h>
#include <stdint.h>

typedef unsigned u32;
typedef unsigned long long u64;
typedef u32 u32x4 __attribute__((ext_vector_type(4)));
typedef u32 u32x2 __attribute__((ext_vector_type(2)));
typedef u32 u32x8 __attribute__((ext_vector_type(8)));
typedef float f32x4 __attribute__((ext_vector_type(4)));
typedef float f32x2 __attribute__((ext_vector_type(2)));
typedef short s16x4 __attribute__((ext_vector_type(4)));
typedef __bf16 bf16x8 __attribute__((ext_vector_type(8)));
typedef __bf16 bf16x2 __attribute__((ext_vector_type(2)));
#define LAS __attribute__((address_space(3)))
#define GAS __attribute__((address_space(1)))
#define DI __device__ __forceinline__
typedef __amdgpu_buffer_rsrc_t rsrc_t;

#define DIAG 0
#define CODEPF 35
#define XB 11
#define ABL_NOLOAD 0
#define ABL_NOCOMP 0

constexpr int QP = 45056;
constexpr int RP = 32768;
constexpr int KS = 135168;
constexpr int P_A = 98304, P_B = 116736, P_C = 135168;
constexpr int MISC = 153600;
constexpr int JUNK = 153856;
constexpr int LDS_BYTES = 154112;
constexpr u32 WS_F1 = 0;
constexpr u32 WS_F3 = 32768;
constexpr u32 WS_R = 65536;
constexpr u32 WS_P = 131072;
constexpr u32 WS_M = WS_P + 256u * 18432u;
constexpr u32 WS_X = WS_M + 1024u * 6144u;
constexpr u32 WS_DIAG = 16u << 20;
constexpr u64 TOKK = 0x9E3779B97F4A7C15ull;
constexpr u32 SPIN_CAP = 1u << 20;

DI rsrc_t mk(const void* p, u32 bytes) { return __builtin_amdgcn_make_buffer_rsrc((void*)p, 0, bytes, 0x00020000); }
DI void dma(rsrc_t r, LAS char* dst, u32 voff, u32 soff) { if (ABL_NOLOAD) return; __builtin_amdgcn_raw_ptr_buffer_load_lds(r, (LAS void*)dst, 16, voff, soff, 0, 0); }
DI void dma_asm(rsrc_t r, u32 dst, u32 voff, u32 soff) {
  if (ABL_NOLOAD) return;
  asm volatile("s_mov_b32 m0, %0\n\ts_nop 0\n\tbuffer_load_dwordx4 %1, %2, %3 offen lds" :: "s"(dst), "v"(voff), "s"(r), "s"(soff) : "memory", "m0");
}
#define WAITV(n) asm volatile("s_waitcnt vmcnt(%0)" ::"i"(n) : "memory")
#define BARRIER() asm volatile("s_barrier" ::: "memory")
#define LBARRIER() asm volatile("s_waitcnt lgkmcnt(0)\n\ts_barrier" ::: "memory")
#define SB() __builtin_amdgcn_sched_barrier(0)
#define PIN(x) asm volatile("" : "+v"(x))
DI int lane_id() { int l = __builtin_amdgcn_mbcnt_hi(~0u, __builtin_amdgcn_mbcnt_lo(~0u, 0u)); PIN(l); return l; }
DI u32 vmcnt_now() { u32 v; asm volatile("s_getreg_b32 %0, hwreg(HW_REG_IB_STS)" : "=s"(v) :: "memory"); return v & 0x00C0000Fu; }
DI f32x4 mfma(u32x4 a, u32x4 b, f32x4 c) { return __builtin_amdgcn_mfma_f32_16x16x32_bf16(__builtin_bit_cast(bf16x8, a), __builtin_bit_cast(bf16x8, b), c, 0, 0, 0); }
DI u32 pk(float a, float b) { f32x2 f = {a, b}; return __builtin_bit_cast(u32, __builtin_convertvector(f, bf16x2)); }
DI u32x2 pk4(f32x4 v) { u32x2 r = {pk(v[0], v[1]), pk(v[2], v[3])}; return r; }
DI u32x4 cat(u32x2 a, u32x2 b) { u32x4 r = {a[0], a[1], b[0], b[1]}; return r; }
DI u32x2 trd(LAS char* p) { return __builtin_bit_cast(u32x2, __builtin_amdgcn_ds_read_tr16_b64_v4i16((LAS s16x4*)p)); }
DI u32x4 lds128(LAS char* p) { return *(LAS u32x4*)p; }
DI u32x2 lds64(LAS char* p) { return *(LAS u32x2*)p; }
DI float bflo(u32 x) { return __builtin_bit_cast(float, x << 16); }
DI float bfhi(u32 x) { return __builtin_bit_cast(float, x & 0xffff0000u); }
DI u64 ld_agent(GAS u64* p) { return __hip_atomic_load(p, __ATOMIC_RELAXED, __HIP_MEMORY_SCOPE_AGENT); }
DI void st_agent(GAS u64* p, u64 v) { __hip_atomic_store(p, v, __ATOMIC_RELAXED, __HIP_MEMORY_SCOPE_AGENT); }
DI u64 rdlane64(u64 v, int l) { return ((u64)(u32)__builtin_amdgcn_readlane((int)(v >> 32), l) << 32) | (u32)__builtin_amdgcn_readlane((int)v, l); }
DI LAS char* vadd(LAS char* p, int d) { u32 r; asm volatile("v_add_u32 %0, %1, %2" : "=v"(r) : "s"(d), "v"((u32)(uintptr_t)p)); return (LAS char*)(uintptr_t)r; }
DI int wlane(int val, int idx, int old) { asm volatile("s_mov_b32 m0, %2\n\ts_nop 0\n\tv_writelane_b32 %0, %1, m0" : "+v"(old) : "s"(val), "s"(idx) : "m0"); return old; }
#define STAMP(i) do {} while (0)

struct Ctx {
  const char *x, *k, *v, *wq, *wo;
  u64 a[7], pc;
  char *out, *ws;
  int stamps; u32 ta, tb;
  int br, rank, bp, j, h0, kvsh;
};

DI void start(Ctx& c, LAS char* sm, int lane, int wv) {
  asm volatile("s_waitcnt lgkmcnt(0)\n\ts_mov_b64 %0, %7\n\ts_mov_b64 %1, %8\n\ts_mov_b64 %2, %9\n\ts_mov_b64 %3, %10\n\ts_mov_b64 %4, %11\n\ts_mov_b64 %5, %12\n\ts_mov_b64 %6, %13"
               : "=&s"(c.x), "=&s"(c.wq), "=&s"(c.k), "=&s"(c.v), "=&s"(c.wo), "=&s"(c.out), "=&s"(c.ws)
               : "s"(c.a[0]), "s"(c.a[1]), "s"(c.a[2]), "s"(c.a[3]), "s"(c.a[4]), "s"(c.a[5]), "s"(c.a[6]) : "memory");
  const rsrc_t rx = mk(c.x, 768 * 1152), rq = mk(c.wq, 576 * 8192);
  const u32 l4 = lane >> 4;
  const u32 vx = (lane >> 3) * 1152 + ((lane & 7) ^ (l4 | ((wv & 1) << 2))) * 16;
  const u32 vq = l4 * 8192 + ((lane & 15) ^ ((l4 | ((wv & 2) << 1)) << 1)) * 16;
  const u32 xs = (u32)(c.bp * 96 + wv * 8) * 1152u, qs = (u32)(c.h0 + 2 * c.j) * 256u + wv * (4 * 8192);
  const u32 d = (u32)(uintptr_t)sm + wv * 1024;
  dma_asm(rx, d, vx, xs);
  dma_asm(rx, (u32)(uintptr_t)sm + (wv >> 2) * QP + (8 + (wv & 3)) * 1024, vx, (u32)(c.bp * 96 + 64 + (wv & 3) * 8) * 1152u + (wv >> 2) * 128);
#pragma unroll
  for (int i = 0; i < 4; ++i) dma_asm(rq, d + 12288 + i * 8192, vq, qs + (i & 1) * (32 * 8192) + (i >> 1) * 256);
  dma_asm(rx, d + QP, vx, xs + 128);
#pragma unroll
  for (int i = 0; i < 4; ++i) dma_asm(rq, d + QP + 12288 + i * 8192, vq, qs + 64 * 8192 + (i & 1) * (32 * 8192) + (i >> 1) * 256);
  STAMP(30);
}

template <int L> struct Loader {
  rsrc_t rx, rq, ro, rv, rk;
  u32 vx[2], vq[2], vo[2], vv[2], ve, vk;
  u32 xso, qso, oso, vso0, vso1, eso, kso[4];
  int hown, h0, kvsh;
  static constexpr int N1 = 4 + (L == 2 ? 10 : 11);
  static constexpr int NK = L == 2 ? 6 : 5;
  static constexpr int N2N = 4 + (L == 0 ? 6 : 5);
  static constexpr int N2E = N2N + (L == 0 ? 0 : 2);
  DI void p1(int ks, LAS char* q) {
    const u32 xs = xso + ks * 128, ws_ = qso + ks * (64 * 8192);
#pragma unroll
    for (int i = 0; i < 4; ++i) { const int p = L + 3 * i; dma(rx, q + p * 1024, vx[p & 1], xs + p * (8 * 1152)); }
#pragma unroll
    for (int i = 0; i < 11; ++i) { const int p = L + 3 * i; if (p < 32) dma(rq, q + 12288 + p * 1024, vq[(p >> 1) & 1], ws_ + (p & 15) * (4 * 8192) + (p >> 4) * 256); }
  }
  DI void pk_(int kt, LAS char* d) {
#pragma unroll
    for (int i = 0; i < 6; ++i) { const int p = (2 - L) + 3 * i; if (p < 16) dma(rk, d + p * 1024, vk ^ ((p & 3) << 6), kso[p >> 2] + kt * 4096 + (p & 3) * 1024); }
  }
  template <bool EXT> DI void p2(int st, LAS char* r) {
    const int h = h0 + ((hown + (st >> 1)) & 7);
    const u32 row = (u32)(h * 128 + (st & 1) * 64) * 1152u;
    const u32 vh = (u32)(h >> kvsh) * (48 * 256) + (st & 1) * 128;
#pragma unroll
    for (int i = 0; i < 4; ++i) { const int p = L + 3 * i; dma(rv, r + 16384 + p * 1024, vv[p & 1], (p < 6 ? vso0 : vso1) + vh + (p % 6) * 2048); }
#pragma unroll
    for (int i = 0; i < 6; ++i) { const int p = L + 3 * i; if (p < 16) dma(ro, r + p * 1024, vo[(p >> 1) & 1], oso + row + p * (4 * 1152)); }
    if (EXT && L > 0) {
#pragma unroll
      for (int i = 0; i < 2; ++i) { const int p = (L - 1) + 2 * i; dma(ro, r + 28672 + p * 1024, ve, eso + row + p * (16 * 1152)); }
    }
  }
};

template <int L> DI void loader(Ctx& c, LAS char* sm) {
  const int lane = lane_id();
  start(c, sm, lane, 4 + L);
  Loader<L> z;
  const int hkv = 32 >> c.kvsh;
  z.rx = mk(c.x, 768 * 1152); z.rq = mk(c.wq, 576 * 8192); z.ro = mk(c.wo, 4096 * 1152);
  z.rv = mk(c.v, (u32)(16 * hkv) * (48 * 256)); z.rk = mk(c.k, (u32)(16 * hkv) * (48 * 256));
  const u32 l3 = lane >> 3, c8 = lane & 7, l4 = lane >> 4, c16 = lane & 15;
  const u32 cx0 = (c8 ^ l4) * 16, cx1 = (c8 ^ (l4 | 4)) * 16, cw0 = (c16 ^ (l4 << 1)) * 16, cw1 = (c16 ^ ((l4 | 4) << 1)) * 16;
  z.vx[0] = l3 * 1152 + cx0; z.vx[1] = l3 * 1152 + cx1;
  z.vq[0] = l4 * 8192 + cw0; z.vq[1] = l4 * 8192 + cw1;
  z.xso = (u32)(c.bp * 96) * 1152u;
  z.hown = 2 * c.j; z.h0 = c.h0; z.kvsh = c.kvsh;
  z.qso = (u32)(c.h0 + z.hown) * 256u;
  z.vo[0] = l4 * 1152 + cw0; z.vo[1] = l4 * 1152 + cw1;
  z.vv[0] = l3 * 256 + cx0; z.vv[1] = l3 * 256 + cx1;
  z.vk = l4 * 256 + (c16 ^ l4) * 16;
  z.ve = (lane >> 2) * 1152 + ((lane & 3) ^ ((lane >> 5) << 1)) * 16;
  z.oso = c.j * 256; z.eso = 1024 + (c.j & 1) * 64;
  z.vso0 = (u32)(2 * c.bp * hkv) * (48 * 256); z.vso1 = z.vso0 + (u32)hkv * (48 * 256);
#pragma unroll
  for (int w = 0; w < 4; ++w) z.kso[w] = ((w >> 1) ? z.vso1 : z.vso0) + (u32)((c.h0 + z.hown + (w & 1)) >> c.kvsh) * (48 * 256);
  STAMP(33); WAITV(5); STAMP(34); BARRIER();
  int slot = 2;
#pragma unroll 1
  for (int s = 2; s < 9; ++s) {
    z.p1(s, sm + slot * QP);
    if (slot == 2) { z.pk_(s / 3, sm + KS); WAITV(Loader<L>::N1 + Loader<L>::NK); slot = 0; } else { WAITV(Loader<L>::N1); ++slot; }
    BARRIER();
  }
#pragma unroll 1
  for (int s = 0; s < 8; ++s) { z.template p2<true>(s, sm + slot * RP); slot = slot == 2 ? 0 : slot + 1; WAITV(Loader<L>::N2E); BARRIER(); }
#pragma unroll 1
  for (int s = 8; s < 16; ++s) { z.template p2<false>(s, sm + slot * RP); slot = slot == 2 ? 0 : slot + 1; WAITV(Loader<L>::N2N); BARRIER(); }
  WAITV(0); BARRIER();
  STAMP(31);
}

struct Pace {
  int g, budget;
  DI void bar() { BARRIER(); ++g; budget = 4; }
  DI void idle(int dead) {
    for (;;) {
      if (vmcnt_now() == 0u) break;
      if (budget > 0) { __builtin_amdgcn_s_sleep(1); --budget; }
      else if (g < dead) bar();
      else break;
    }
    WAITV(0);
  }
};
DI u64 ld_async(const char* base, u32 off) { u64 r; asm volatile("global_load_dwordx2 %0, %1, %2 sc1" : "=v"(r) : "v"(off), "s"(base) : "memory"); return r; }

DI void comm(Ctx& c, LAS char* sm) {
  const int lane = lane_id();
  start(c, sm, lane, 7);
  char* ws = c.ws;
  if (CODEPF) {
    u32 line = ((blockIdx.x >> 3) % 5) * 64 + lane; line = line < CODEPF * 8 ? line : 0u;
    asm volatile("s_mov_b32 m0, %0\n\ts_nop 0\n\tglobal_load_lds_dword %1, %2" :: "s"((u32)(uintptr_t)sm + JUNK), "v"(line * 128), "s"(c.pc & ~255ull) : "memory", "m0");
  }
  const int sg = c.br * 8 + c.bp, me = c.rank * 4 + c.j, m = lane & 15;
  const char* F1 = ws + WS_F1 + sg * 2048;
  const char* R = ws + WS_R + sg * 2048;
  Pace pc;
  __builtin_amdgcn_s_setprio(3);
  STAMP(33); WAITV(5 + (CODEPF ? 1 : 0)); STAMP(34); BARRIER();
  u64 tok = ld_async(F1, m * 128);
  WAITV(1);
  pc.g = 1; pc.bar();
  pc.idle(6); PIN(tok);
  tok += TOKK;
  if (lane < 16) { st_agent((GAS u64*)(R + m * 128 + me * 8), tok); *(LAS u64*)(sm + MISC + m * 8) = tok; }
  if (lane == 0) *(LAS u32*)(sm + MISC + 128) = 0u;
  const u64 mytok = rdlane64(tok, me);
  asm volatile("s_waitcnt lgkmcnt(0)" ::: "memory");
  while (pc.g < 8) pc.bar();
  for (u32 spins = 0; spins < SPIN_CAP; ++spins) {
    u64 r = ld_async(R, me * 128 + m * 8);
    pc.idle(XB + 1); PIN(r);
    if (__all(r == mytok)) break;
  }
  while (pc.g <= XB) pc.bar();
  if (lane == 0) st_agent((GAS u64*)(F1 + me * 128), mytok);
  STAMP(40);
  const rsrc_t rp = mk(ws + WS_P, 256u * 18432u);
  const u32 foff = (u32)(c.rank * 4 + (lane & 3)) * 128;
  const u64 want = __shfl(tok, c.rank * 4 + (lane & 3));
  u32 seen = 0;
#pragma unroll 1
  for (int t = 1; t < 4; ++t) {
    const int pj = (c.j + t) & 3, dead = 13 + 4 * t;
    for (u32 spins = 0; !((seen >> pj) & 1u) && spins < SPIN_CAP; ++spins) {
      u64 r = ld_async(F1, foff);
      do { if (pc.g < dead - 1) pc.bar(); } while (vmcnt_now() != 0u);
      WAITV(0); PIN(r);
      seen = (u32)__ballot(r == want) & 15u;
    }
    STAMP(40 + 2 * t - 1);
    const u32 so = (u32)((c.bp * 4 + pj) * 8 + c.rank * 2 + c.br) * 18432u;
    u32x4 pv[18];
#pragma unroll
    for (int i = 0; i < 18; ++i) asm volatile("buffer_load_dwordx4 %0, %1, %2, %3 offen offset:%4 sc1" : "=v"(pv[i]) : "v"(lane * 16 + (i >> 2) * 4096), "s"(rp), "s"(so), "i"((i & 3) * 1024) : "memory");
    STAMP(47 + 3 * t);
    do { if (pc.g < dead) pc.bar(); } while (vmcnt_now() != 0u || (t == 3 && pc.g <= 14));
    WAITV(0);
    STAMP(48 + 3 * t);
    LAS char* dst = sm + (t == 1 ? P_B : t == 2 ? P_C : P_A) + lane * 16;
#pragma unroll
    for (int i = 0; i < 18; ++i) { PIN(pv[i]); *(LAS u32x4*)(dst + i * 1024) = pv[i]; }
    asm volatile("s_waitcnt lgkmcnt(0)" ::: "memory");
    STAMP(40 + 2 * t);
    if (pc.g <= dead) pc.bar();
  }
  while (pc.g <= 25) pc.bar();
}

template <bool ADV> DI void half1(f32x4 (&Q)[8][3], const u32x4 (&A)[8], const u32x4 (&B)[3], u32x4 (&An)[8], u32x4 (&Bn)[3],
                                  LAS char* (&w)[8], LAS char* (&x)[2], int u, int adv) {
  u32x2 lo[8];
#pragma unroll
  for (int i = 0; i < 24; ++i) {
    Q[i / 3][i % 3] = mfma(A[i / 3], B[i % 3], Q[i / 3][i % 3]);
    constexpr int what[19] = {70, 71, 00, 01, 100, 10, 11, 101, 20, 21, 102, 30, 31, 60, 40, 41, 61, 50, 51};
    if (i < 19) {
      const int k = what[i];
      if (k >= 100) Bn[k - 100] = lds128(x[u] + (k - 100) * 2048);
      else if (k % 10 == 0) lo[k / 10] = trd(w[k / 10] + u * 8192);
      else An[k / 10] = cat(lo[k / 10], trd(w[k / 10] + u * 8192 + 1024));
    } else if (ADV) {
      if (i < 23) { w[2 * (i - 19)] = vadd(w[2 * (i - 19)], adv); w[2 * (i - 19) + 1] = vadd(w[2 * (i - 19) + 1], adv); }
      else { x[0] = vadd(x[0], adv); x[1] = vadd(x[1], adv); }
    }
    SB();
  }
}
struct G2 { u32x4 v[3], w[5]; u32x2 lo; };
struct A2 { LAS char* w[4]; LAS char* e; LAS char* v[2]; };
DI void shift(A2& a, int d) {
#pragma unroll
  for (int i = 0; i < 4; ++i) a.w[i] = vadd(a.w[i], d);
  a.e = vadd(a.e, d); a.v[0] = vadd(a.v[0], d); a.v[1] = vadd(a.v[1], d);
}
template <bool EXT> DI void rd2(int i, G2& f, const A2& a, int off, int u) {
  if (i < 3) f.v[i] = lds128(a.v[u] + off + 16384 + i * 2048);
  else if (i < 11) { const int ct = (i - 3) >> 1; if ((i - 3) & 1) f.w[ct] = cat(f.lo, trd(a.w[ct] + off + u * 8192 + 1024)); else f.lo = trd(a.w[ct] + off + u * 8192); }
  else if (EXT && i == 11) f.lo = trd(a.e + off + 28672 + u * 2048);
  else if (EXT && i == 12) f.w[4] = cat(f.lo, trd(a.e + off + 28672 + u * 2048 + 256));
}
template <bool E, bool FIRST, bool READ, bool PACK, int DBL>
DI void step2(f32x4 (&U)[3][5], const G2& f, G2& fn, const A2& a, int off, int u, u32x4 (&UA)[5], u32x2 (&UC)[5]) {
  const f32x4 z = {0.f, 0.f, 0.f, 0.f};
#pragma unroll
  for (int i = 0; i < (E ? 15 : 12); ++i) {
    const int ct = i / 3, kt = i % 3;
    if (PACK) { const u32x2 t = pk4(U[kt][ct]); if (kt == 0) { UA[ct][0] = t[0]; UA[ct][1] = t[1]; } else if (kt == 1) { UA[ct][2] = t[0]; UA[ct][3] = t[1]; } else UC[ct] = t; SB(); }
    U[kt][ct] = mfma(f.v[kt], f.w[ct], FIRST ? z : U[kt][ct]);
    if (READ) { if (i < DBL) { rd2<E>(2 * i, fn, a, off, u); rd2<E>(2 * i + 1, fn, a, off, u); } else rd2<E>(i + DBL, fn, a, off, u); }
    SB();
  }
}
template <bool PE, bool CE, bool READ, bool FIRST>
DI void finish2(f32x4 (&O)[5][3], f32x4 (&U)[3][5], const u32x4 (&A0)[5], const u32x2 (&C0)[5], G2& f0, G2& f1, const A2& a, int off, LAS char* p, LAS char* pc) {
  constexpr int N = PE ? 15 : 12;
  u32x4 pa0[3], pa1[3], pcc[3], A1[5]; u32x2 C1[5], lo[3];
#pragma unroll
  for (int i = 0; i < N; ++i) {
    U[i % 3][i / 3] = mfma(f1.v[i % 3], f1.w[i / 3], U[i % 3][i / 3]);
    if (i < 3) pa0[i] = lds128(p + i * 1024);
    else if (i < 6) pa1[i - 3] = lds128(p + 4608 + (i - 3) * 1024);
    else if (READ) rd2<CE>(i - 6, f0, a, off, 0);
    SB();
  }
#pragma unroll
  for (int i = 0; i < N; ++i) {
    const int ct = i / 3, n = i % 3;
    O[ct][n] = mfma(A0[ct], pa0[n], FIRST ? (f32x4){0.f, 0.f, 0.f, 0.f} : O[ct][n]);
    const u32x2 t = pk4(U[n][ct]);
    if (n == 0) { A1[ct][0] = t[0]; A1[ct][1] = t[1]; } else if (n == 1) { A1[ct][2] = t[0]; A1[ct][3] = t[1]; } else C1[ct] = t;
    SB();
  }
#pragma unroll
  for (int i = 0; i < N; ++i) {
    O[i / 3][i % 3] = mfma(A1[i / 3], pa1[i % 3], O[i / 3][i % 3]);
    if (i < 3) lo[i] = lds64(pc + i * 512);
    else if (i < 6) pcc[i - 3] = cat(lo[i - 3], lds64(pc + 4608 + (i - 3) * 512));
    else if (READ) rd2<CE>(i + N - 12, f0, a, off, 0);
    SB();
  }
  if (READ && N == 12) rd2<CE>(12, f0, a, off, 0);
#pragma unroll
  for (int i = 0; i < N; ++i) {
    O[i / 3][i % 3] = mfma(cat(C0[i / 3], C1[i / 3]), pcc[i % 3], O[i / 3][i % 3]);
    if (READ) rd2<CE>(i, f1, a, off, 1);
    SB();
  }
  if (READ && N == 12) rd2<CE>(12, f1, a, off, 1);
}
DI float max3(float a, float b, float c) { float r; asm("v_max3_f32 %0, %1, %2, %3" : "=v"(r) : "v"(a), "v"(b), "v"(c)); return r; }
DI float max2(float a, float b) { float r; asm("v_max_f32 %0, %1, %2" : "=v"(r) : "v"(a), "v"(b)); return r; }

DI void tail(Ctx& c, LAS char* sm, int wv, char* ws, char* out, f32x4 (&O)[5][3], bool xown) {
  const int lane = lane_id();
  const int hl = wv & 1, g = lane >> 4, m = lane & 15, b = 2 * c.bp + (wv >> 1);
  const u32 cu = blockIdx.x;
  const int sg = c.br * 8 + c.bp, me = c.rank * 4 + c.j;
  GAS u64* F3 = (GAS u64*)(ws + WS_F3) + sg * 256;
  const rsrc_t rm = mk(ws + WS_M, 1024u * 6144u);
  if (wv != c.rank) {
#pragma unroll
    for (int n = 0; n < 3; ++n)
#pragma unroll
      for (int c2 = 0; c2 < 2; ++c2) __builtin_amdgcn_raw_buffer_store_b128(cat(pk4(O[2 * c2][n]), pk4(O[2 * c2 + 1][n])), rm, lane * 16 + (n * 2 + c2) * 1024, (cu * 4 + wv) * 6144u, 16);
  }
  WAITV(0);
  STAMP(28);
  const u64 tok = *(LAS u64*)(sm + MISC + m * 8);
  {
    u32 old = 0;
    if (lane == 0) old = __hip_atomic_fetch_add((LAS u32*)(sm + MISC + 128), 1u, __ATOMIC_RELAXED, __HIP_MEMORY_SCOPE_WORKGROUP);
    old = __builtin_amdgcn_readfirstlane(old);
    if (old == 3u && lane == me) st_agent(F3 + me * 16, tok);
  }
  if (wv != c.rank) return;
  {
    const bool need = m != me && ((m & 3) == c.j || (xown && (m & 3) == (c.j ^ 2)));
    u32 spins = 0;
    while (!__all(!need || ld_agent(F3 + m * 16) == tok) && ++spins < SPIN_CAP) __builtin_amdgcn_s_sleep(1);
  }
  STAMP(29);
  u32x4 in[3][6];
#pragma unroll
  for (int r = 0; r < 3; ++r) {
    const u32 pr = (u32)((c.rank + 1 + r) & 3), pcu = (cu & ~6u) | (pr << 1);
#pragma unroll
    for (int i = 0; i < 6; ++i) in[r][i] = __builtin_amdgcn_raw_buffer_load_b128(rm, lane * 16 + i * 1024, (pcu * 4 + wv) * 6144u, 16);
  }
  const rsrc_t ro = mk(out, 768 * 2304);
  const u32 orow = (u32)(b * 48 + m) * 2304u + c.br * 1152;
  if (xown) {
    const rsrc_t rxx = mk(ws + WS_X, 1024u * 2048u);
    u32x2 xin[7][3];
#pragma unroll
    for (int r = 0; r < 7; ++r) {
      const u32 pr = (u32)((c.rank + 1 + r) & 3), pcu = ((cu & ~6u) | (pr << 1)) ^ (r >= 3 ? 16u : 0u);
#pragma unroll
      for (int n = 0; n < 3; ++n) xin[r][n] = __builtin_amdgcn_raw_buffer_load_b64(rxx, lane * 8 + n * 512, (pcu * 4 + wv) * 2048u, 16);
    }
#pragma unroll
    for (int n = 0; n < 3; ++n) {
      f32x4 a = O[4][n];
#pragma unroll
      for (int r = 0; r < 7; ++r) a += (f32x4){bflo(xin[r][n][0]), bfhi(xin[r][n][0]), bflo(xin[r][n][1]), bfhi(xin[r][n][1])};
      __builtin_amdgcn_raw_buffer_store_b64(pk4(a), ro, orow + n * (16 * 2304) + (512 + 32 * (c.j & 1) + 16 * hl + 4 * g) * 2, 0, 0);
    }
  }
#pragma unroll
  for (int n = 0; n < 3; ++n)
#pragma unroll
    for (int c2 = 0; c2 < 2; ++c2) {
      f32x4 a = O[2 * c2][n], d = O[2 * c2 + 1][n];
#pragma unroll
      for (int r = 0; r < 3; ++r) {
        const u32x4 v = in[r][n * 2 + c2];
        a += (f32x4){bflo(v[0]), bfhi(v[0]), bflo(v[1]), bfhi(v[1])};
        d += (f32x4){bflo(v[2]), bfhi(v[2]), bflo(v[3]), bfhi(v[3])};
      }
      const u32x2 pa = pk4(a), pd = pk4(d);
      auto s0 = __builtin_amdgcn_permlane16_swap(pa[0], pd[0], false, false);
      auto s1 = __builtin_amdgcn_permlane16_swap(pa[1], pd[1], false, false);
      __builtin_amdgcn_raw_buffer_store_b128((u32x4){s0[0], s1[0], s0[1], s1[1]}, ro,
          orow + n * (16 * 2304) + (128 * c.j + 64 * hl + 32 * c2 + 16 * (g & 1) + 8 * (g >> 1)) * 2, 0, 0);
    }
  STAMP(32);
}

DI void compute(Ctx& c, LAS char* sm, int wv) {
  start(c, sm, lane_id(), wv);
  char* ws = c.ws;
  const int bl = wv >> 1, hl = wv & 1;
  const u32 cu = blockIdx.x;
  int lane = lane_id(), g = lane >> 4, m = lane & 15, q = m >> 2, p = lane & 3;
  u32 wbase = (8 * g + q) * 256 + 32 * (q | ((g & 1) << 2)) + 8 * p;
  u32 xbase = (bl * 48 + m) * 128 + ((g ^ (m >> 1)) * 16);
  u32x4 Kf[3][4];
  f32x4 Q[8][3];
  u32x4 A1[8], B1[3];
  LAS char* aw[8]; LAS char* ax[2];
#pragma unroll
  for (int T = 0; T < 8; ++T) { A1[T] = (u32x4){0, 0, 0, 0}; PIN(A1[T]);
    aw[T] = vadd(sm + (wbase ^ (T << 5)), 12288 + hl * 16384);
#pragma unroll
    for (int n = 0; n < 3; ++n) { Q[T][n] = (f32x4){0.f, 0.f, 0.f, 0.f}; PIN(Q[T][n]); } }
#pragma unroll
  for (int n = 0; n < 3; ++n) { B1[n] = (u32x4){0, 0, 0, 0}; PIN(B1[n]); }
  ax[0] = vadd(sm + xbase, 0); ax[1] = vadd(sm + (xbase ^ 64), 0);
  SB();
  STAMP(33); WAITV(5); STAMP(34); BARRIER();
  STAMP(0);
#pragma unroll
  for (int kt = 0; kt < 3; ++kt) {
#pragma unroll 1
    for (int s = 0; s < 3; ++s) {
      if (s == 2) {
        const u32 l = lane_id(), kb = KS + wv * 4096 + (l & 15) * 256 + ((l >> 5) ^ (l & 15)) * 16 + ((l >> 1) & 8);
#pragma unroll
        for (int i = 0; i < 4; ++i) Kf[kt][i] = cat(lds64(sm + (kb ^ (i << 6))), lds64(sm + (kb ^ (i << 6) ^ 32)));
      }
      u32x4 A0[8], B0[3];
      half1<false>(Q, A1, B1, A0, B0, aw, ax, 0, 0);
      half1<true>(Q, A0, B0, A1, B1, aw, ax, 1, s == 2 ? -2 * QP : QP);
      if (kt == 0 && s == 0) WAITV(0);
      LBARRIER();
      STAMP(1 + 3 * kt + s);
    }
  }
  SB();
  lane = lane_id(); g = lane >> 4; m = lane & 15; q = m >> 2; p = lane & 3;
  wbase = (8 * g + q) * 256 + 32 * (q | ((g & 1) << 2)) + 8 * p;
  xbase = (bl * 48 + m) * 128 + ((g ^ (m >> 1)) * 16);
  A2 a;
#pragma unroll
  for (int ct = 0; ct < 4; ++ct) a.w[ct] = sm + (wbase ^ ((4 * hl + ct) << 5));
  a.e = sm + (8 * g + q) * 64 + 32 * (hl ^ (g & 1)) + 8 * p;
  a.v[0] = sm + xbase; a.v[1] = sm + (xbase ^ 64);
  G2 f0, f1;
  f32x4 O[5][3], U[3][5];
  {
    u32x4 qf[3][4];
    f32x4 S[3][3];
#pragma unroll
    for (int i = 0; i < 24; ++i) {
      Q[i & 7][i >> 3] = mfma(A1[i & 7], B1[i >> 3], Q[i & 7][i >> 3]);
      if (i >= 8) { const u32x2 t = pk4(Q[i & 7][(i >> 3) - 1]); qf[(i >> 3) - 1][(i & 7) >> 1][2 * (i & 1)] = t[0]; qf[(i >> 3) - 1][(i & 7) >> 1][2 * (i & 1) + 1] = t[1]; }
      SB();
    }
    STAMP(36);
#pragma unroll
    for (int i = 0; i < 36; ++i) {
      const int n = i / 12, s = (i % 12) / 3, kt = i % 3;
      S[n][kt] = mfma(Kf[kt][s], qf[n][s], s == 0 ? (f32x4){0.f, 0.f, 0.f, 0.f} : S[n][kt]);
      if (i >= 2 && i < 10) { const u32x2 t = pk4(Q[i - 2][2]); qf[2][(i - 2) >> 1][2 * (i & 1)] = t[0]; qf[2][(i - 2) >> 1][2 * (i & 1) + 1] = t[1]; }
      if (i >= 10 && i < 23) rd2<true>(i - 10, f0, a, 0, 0);
      if (i >= 23) rd2<true>(i - 23, f1, a, 0, 1);
      SB();
    }
    STAMP(37);
    const rsrc_t rp = mk(ws + WS_P, 256u * 18432u);
    const u32 pso = cu * 18432u + wv * 4608;
    LAS char* pl = vadd(sm + lane * 16, P_A + wv * 4608);
    LAS char* pl2 = vadd(sm + lane * 8, P_A + wv * 4608 + 3072);
    float mx[3], sum[3];
    int um = 0;
#define UM() do { if (um < 15) { U[um % 3][um / 3] = mfma(f0.v[um % 3], f0.w[um / 3], (f32x4){0.f, 0.f, 0.f, 0.f}); ++um; } SB(); } while (0)
#pragma unroll
    for (int n = 0; n < 3; ++n)
      mx[n] = max3(max3(S[n][0][0], S[n][0][1], S[n][0][2]), max3(S[n][0][3], S[n][1][0], S[n][1][1]),
                   max3(max3(S[n][1][2], S[n][1][3], S[n][2][0]), max3(S[n][2][1], S[n][2][2], S[n][2][3]), S[n][0][0]));
#pragma unroll
    for (int n = 0; n < 3; ++n) { auto r = __builtin_amdgcn_permlane16_swap(__float_as_uint(mx[n]), __float_as_uint(mx[n]), false, false); mx[n] = max2(__uint_as_float(r[0]), __uint_as_float(r[1])); }
#pragma unroll
    for (int n = 0; n < 3; ++n) { auto r = __builtin_amdgcn_permlane32_swap(__float_as_uint(mx[n]), __float_as_uint(mx[n]), false, false); mx[n] = max2(__uint_as_float(r[0]), __uint_as_float(r[1])) * -0.12751743344863963f; }
    SB();
#pragma unroll
    for (int n = 0; n < 3; ++n)
#pragma unroll
      for (int kt = 0; kt < 3; ++kt) {
#pragma unroll
        for (int i = 0; i < 4; ++i) S[n][kt][i] = __builtin_amdgcn_exp2f(__builtin_fmaf(S[n][kt][i], 0.12751743344863963f, mx[n]));
        UM();
      }
    STAMP(38);
#pragma unroll
    for (int n = 0; n < 3; ++n) { const f32x4 s4 = S[n][0] + S[n][1] + S[n][2]; sum[n] = (s4[0] + s4[1]) + (s4[2] + s4[3]); UM(); }
#pragma unroll
    for (int n = 0; n < 3; ++n) { auto r = __builtin_amdgcn_permlane16_swap(__float_as_uint(sum[n]), __float_as_uint(sum[n]), false, false); sum[n] = __uint_as_float(r[0]) + __uint_as_float(r[1]); }
#pragma unroll
    for (int n = 0; n < 3; ++n) { auto r = __builtin_amdgcn_permlane32_swap(__float_as_uint(sum[n]), __float_as_uint(sum[n]), false, false); sum[n] = __builtin_amdgcn_rcpf(__uint_as_float(r[0]) + __uint_as_float(r[1])); }
    SB();
    STAMP(39);
#pragma unroll
    for (int n = 0; n < 3; ++n) {
      const u32x4 pa = cat(pk4(S[n][0] * sum[n]), pk4(S[n][1] * sum[n]));
      const u32x2 pc = pk4(S[n][2] * sum[n]);
      __builtin_amdgcn_raw_buffer_store_b128(pa, rp, lane * 16 + n * 1024, pso, 16);
      __builtin_amdgcn_raw_buffer_store_b64(pc, rp, lane * 8 + 3072 + n * 512, pso, 16);
      *(LAS u32x4*)(pl + n * 1024) = pa;
      *(LAS u32x2*)(pl2 + n * 512) = pc;
      UM();
    }
  }
  STAMP(26);
  SB();
  LAS char* pp = sm + bl * 9216 + lane * 16;
  LAS char* pq = sm + bl * 9216 + lane * 8 + 3072;
  u32x4 UA[5]; u32x2 UC[5];
  const bool xown = (wv == c.rank) && ((c.j >> 1) == (c.rank & 1));
#define ENDI(gi) do { if ((gi) % 3 == 0) shift(a, RP); if ((gi) % 3 == 2) shift(a, -RP); if ((gi) + 1 == XB) WAITV(0); LBARRIER(); STAMP(1 + (gi)); SB(); } while (0)
#define SLOT(gi) ((gi) % 3 == 2 ? RP : 0)
#define BODY(t, PE, CE) do { \
    constexpr int g0 = 9 + 4 * (t); \
    if ((t) > 0) { \
      constexpr int pa_ = (t) == 1 ? P_A : (t) == 2 ? P_B : P_C; \
      finish2<PE, CE, true, (t) == 1>(O, U, UA, UC, f0, f1, a, SLOT(g0), vadd(pp, pa_), vadd(pq, pa_)); \
      if ((t) == 2 && !xown) { const rsrc_t rxx = mk(ws + WS_X, 1024u * 2048u); \
        _Pragma("unroll") for (int n = 0; n < 3; ++n) __builtin_amdgcn_raw_buffer_store_b64(pk4(O[4][n]), rxx, lane * 8 + n * 512, (cu * 4 + wv) * 2048u, 16); } \
      step2<CE, true, false, false, 0>(U, f0, f1, a, 0, 0, UA, UC); } \
    ENDI(g0); \
    step2<CE, false, true, false, 0>(U, f1, f0, a, SLOT(g0 + 1), 0, UA, UC); \
    step2<CE, false, true, false, 3>(U, f0, f1, a, SLOT(g0 + 1), 1, UA, UC); ENDI(g0 + 1); \
    step2<CE, false, true, false, 0>(U, f1, f0, a, SLOT(g0 + 2), 0, UA, UC); \
    step2<CE, true, true, true, 3>(U, f0, f1, a, SLOT(g0 + 2), 1, UA, UC); ENDI(g0 + 2); \
    step2<CE, false, true, false, 0>(U, f1, f0, a, SLOT(g0 + 3), 0, UA, UC); \
    step2<CE, false, true, false, 3>(U, f0, f1, a, SLOT(g0 + 3), 1, UA, UC); ENDI(g0 + 3); \
  } while (0)
  BODY(0, true, true);
  BODY(1, true, true);
  BODY(2, true, false);
  BODY(3, false, false);
  finish2<false, false, false, false>(O, U, UA, UC, f0, f1, a, 0, vadd(pp, P_A), vadd(pq, P_A));
  STAMP(27);
  tail(c, sm, wv, ws, c.out, O, xown);
}

__global__ __launch_bounds__(512, 2) void fused_attn_block(const char* xA, const char* kA, const char* vA, const char* wqA, const char* woA,
                                                          const char* xB, const char* kB, const char* vB, const char* wqB, const char* woB,
                                                          char* out, char* ws) {
  __shared__ __attribute__((aligned(256))) char smem[LDS_BYTES];
  LAS char* sm = (LAS char*)smem;
  const int wv = __builtin_amdgcn_readfirstlane(threadIdx.x >> 6);
  Ctx c;
  c.stamps = 0; c.ta = 0; c.tb = 0;
  STAMP(63);
  const int id = blockIdx.x;
  c.br = id & 1; c.rank = (id >> 1) & 3; c.j = (id >> 3) & 3; c.bp = id >> 5;
  c.h0 = c.rank * 8; c.kvsh = 1 - c.br;
  {
    const u64 ka = (u64)(uintptr_t)__builtin_amdgcn_kernarg_segment_ptr();
    asm volatile("s_load_dwordx2 %0, %8, 0x0\n\ts_load_dwordx2 %1, %8, 0x18\n\ts_load_dwordx2 %2, %8, 0x8\n\ts_load_dwordx2 %3, %8, 0x10\n\t"
                 "s_load_dwordx2 %4, %8, 0x20\n\ts_load_dwordx2 %5, %9, 0x50\n\ts_load_dwordx2 %6, %9, 0x58\n\ts_getpc_b64 %7"
                 : "=&s"(c.a[0]), "=&s"(c.a[1]), "=&s"(c.a[2]), "=&s"(c.a[3]), "=&s"(c.a[4]), "=&s"(c.a[5]), "=&s"(c.a[6]), "=&s"(c.pc)
                 : "s"(ka + c.br * 40), "s"(ka) : "memory");
  }
  if (wv == 4) loader<0>(c, sm);
  else if (wv == 5) loader<1>(c, sm);
  else if (wv == 6) loader<2>(c, sm);
  else if (wv == 7) comm(c, sm);
  else if (ABL_NOCOMP) { start(c, sm, lane_id(), wv); STAMP(33); WAITV(5); STAMP(34); BARRIER(); STAMP(0); WAITV(0); for (int g = 1; g < 26; ++g) { BARRIER(); STAMP(g); } }
  else compute(c, sm, wv);
}

extern "C" void kernel_launch(void* const* d_in, const int* in_sizes, int n_in, void* d_out, int out_size, void* d_ws, size_t ws_size,
                              hipStream_t stream) {
  const char* const* in = (const char* const*)d_in;
  fused_attn_block<<<256, 512, 0, stream>>>(in[0], in[1], in[2], in[3], in[4], in[5], in[6], in[7], in[8], in[9], (char*)d_out, (char*)d_ws);
}
```

```cpp
#include <hip/hip_runtime.h>
#include <stdint.h>

typedef unsigned u32;
typedef unsigned long long u64;
typedef u32 u32x4 __attribute__((ext_vector_type(4)));
typedef u32 u32x2 __attribute__((ext_vector_type(2)));
typedef u32 u32x8 __attribute__((ext_vector_type(8)));
typedef float f32x4 __attribute__((ext_vector_type(4)));
typedef float f32x2 __attribute__((ext_vector_type(2)));
typedef short s16x4 __attribute__((ext_vector_type(4)));
typedef __bf16 bf16x8 __attribute__((ext_vector_type(8)));
typedef __bf16 bf16x2 __attribute__((ext_vector_type(2)));
#define LAS __attribute__((address_space(3)))
#define GAS __attribute__((address_space(1)))
#define DI __device__ __forceinline__
typedef __amdgpu_buffer_rsrc_t rsrc_t;

#define DIAG 0
#define CODEPF 35
#define XB 11
#define ABL_NOLOAD 0
#define ABL_NOCOMP 0

constexpr int QP = 45056;
constexpr int RP = 32768;
constexpr int KS = 135168;
constexpr int P_A = 98304, P_B = 116736, P_C = 135168;
constexpr int MISC = 153600;
constexpr int JUNK = 153856;
constexpr int LDS_BYTES = 154112;
constexpr u32 WS_F1 = 0;
constexpr u32 WS_F3 = 32768;
constexpr u32 WS_R = 65536;
constexpr u32 WS_P = 131072;
constexpr u32 WS_M = WS_P + 256u * 18432u;
constexpr u32 WS_X = WS_M + 1024u * 6144u;
constexpr u32 WS_DIAG = 16u << 20;
constexpr u64 TOKK = 0x9E3779B97F4A7C15ull;
constexpr u32 SPIN_CAP = 1u << 20;

DI rsrc_t mk(const void* p, u32 bytes) { return __builtin_amdgcn_make_buffer_rsrc((void*)p, 0, bytes, 0x00020000); }
DI void dma(rsrc_t r, LAS char* dst, u32 voff, u32 soff) { if (ABL_NOLOAD) return; __builtin_amdgcn_raw_ptr_buffer_load_lds(r, (LAS void*)dst, 16, voff, soff, 0, 0); }
DI void dma_asm(rsrc_t r, u32 dst, u32 voff, u32 soff) {
  if (ABL_NOLOAD) return;
  asm volatile("s_mov_b32 m0, %0\n\ts_nop 0\n\tbuffer_load_dwordx4 %1, %2, %3 offen lds" :: "s"(dst), "v"(voff), "s"(r), "s"(soff) : "memory", "m0");
}
#define WAITV(n) asm volatile("s_waitcnt vmcnt(%0)" ::"i"(n) : "memory")
#define BARRIER() asm volatile("s_barrier" ::: "memory")
#define LBARRIER() asm volatile("s_waitcnt lgkmcnt(0)\n\ts_barrier" ::: "memory")
#define SB() __builtin_amdgcn_sched_barrier(0)
#define PIN(x) asm volatile("" : "+v"(x))
DI int lane_id() { int l = __builtin_amdgcn_mbcnt_hi(~0u, __builtin_amdgcn_mbcnt_lo(~0u, 0u)); PIN(l); return l; }
DI u32 vmcnt_now() { u32 v; asm volatile("s_getreg_b32 %0, hwreg(HW_REG_IB_STS)" : "=s"(v) :: "memory"); return v & 0x00C0000Fu; }
DI f32x4 mfma(u32x4 a, u32x4 b, f32x4 c) { return __builtin_amdgcn_mfma_f32_16x16x32_bf16(__builtin_bit_cast(bf16x8, a), __builtin_bit_cast(bf16x8, b), c, 0, 0, 0); }
DI u32 pk(float a, float b) { f32x2 f = {a, b}; return __builtin_bit_cast(u32, __builtin_convertvector(f, bf16x2)); }
DI u32x2 pk4(f32x4 v) { u32x2 r = {pk(v[0], v[1]), pk(v[2], v[3])}; return r; }
DI u32x4 cat(u32x2 a, u32x2 b) { u32x4 r = {a[0], a[1], b[0], b[1]}; return r; }
DI u32x2 trd(LAS char* p) { return __builtin_bit_cast(u32x2, __builtin_amdgcn_ds_read_tr16_b64_v4i16((LAS s16x4*)p)); }
DI u32x4 lds128(LAS char* p) { return *(LAS u32x4*)p; }
DI u32x2 lds64(LAS char* p) { return *(LAS u32x2*)p; }
DI float bflo(u32 x) { return __builtin_bit_cast(float, x << 16); }
DI float bfhi(u32 x) { return __builtin_bit_cast(float, x & 0xffff0000u); }
DI u64 ld_agent(GAS u64* p) { return __hip_atomic_load(p, __ATOMIC_RELAXED, __HIP_MEMORY_SCOPE_AGENT); }
DI void st_agent(GAS u64* p, u64 v) { __hip_atomic_store(p, v, __ATOMIC_RELAXED, __HIP_MEMORY_SCOPE_AGENT); }
DI u64 rdlane64(u64 v, int l) { return ((u64)(u32)__builtin_amdgcn_readlane((int)(v >> 32), l) << 32) | (u32)__builtin_amdgcn_readlane((int)v, l); }
DI LAS char* vadd(LAS char* p, int d) { u32 r; asm volatile("v_add_u32 %0, %1, %2" : "=v"(r) : "s"(d), "v"((u32)(uintptr_t)p)); return (LAS char*)(uintptr_t)r; }
DI int wlane(int val, int idx, int old) { asm volatile("s_mov_b32 m0, %2\n\ts_nop 0\n\tv_writelane_b32 %0, %1, m0" : "+v"(old) : "s"(val), "s"(idx) : "m0"); return old; }
#define STAMP(i) do {} while (0)

struct Ctx {
  const char *x, *k, *v, *wq, *wo;
  u64 a[7], pc;
  char *out, *ws;
  int stamps; u32 ta, tb;
  int br, rank, bp, j, h0, kvsh;
};

DI void start(Ctx& c, LAS char* sm, int lane, int wv) {
  asm volatile("s_waitcnt lgkmcnt(0)\n\ts_mov_b64 %0, %7\n\ts_mov_b64 %1, %8\n\ts_mov_b64 %2, %9\n\ts_mov_b64 %3, %10\n\ts_mov_b64 %4, %11\n\ts_mov_b64 %5, %12\n\ts_mov_b64 %6, %13"
               : "=&s"(c.x), "=&s"(c.wq), "=&s"(c.k), "=&s"(c.v), "=&s"(c.wo), "=&s"(c.out), "=&s"(c.ws)
               : "s"(c.a[0]), "s"(c.a[1]), "s"(c.a[2]), "s"(c.a[3]), "s"(c.a[4]), "s"(c.a[5]), "s"(c.a[6]) : "memory");
  const rsrc_t rx = mk(c.x, 768 * 1152), rq = mk(c.wq, 576 * 8192);
  const u32 l4 = lane >> 4;
  const u32 vx = (lane >> 3) * 1152 + ((lane & 7) ^ (l4 | ((wv & 1) << 2))) * 16;
  const u32 vq = l4 * 8192 + ((lane & 15) ^ ((l4 | ((wv & 2) << 1)) << 1)) * 16;
  const u32 xs = (u32)(c.bp * 96 + wv * 8) * 1152u, qs = (u32)(c.h0 + 2 * c.j) * 256u + wv * (4 * 8192);
  const u32 d = (u32)(uintptr_t)sm + wv * 1024;
  dma_asm(rx, d, vx, xs);
  dma_asm(rx, (u32)(uintptr_t)sm + (wv >> 2) * QP + (8 + (wv & 3)) * 1024, vx, (u32)(c.bp * 96 + 64 + (wv & 3) * 8) * 1152u + (wv >> 2) * 128);
#pragma unroll
  for (int i = 0; i < 4; ++i) dma_asm(rq, d + 12288 + i * 8192, vq, qs + (i & 1) * (32 * 8192) + (i >> 1) * 256);
  dma_asm(rx, d + QP, vx, xs + 128);
#pragma unroll
  for (int i = 0; i < 4; ++i) dma_asm(rq, d + QP + 12288 + i * 8192, vq, qs + 64 * 8192 + (i & 1) * (32 * 8192) + (i >> 1) * 256);
  STAMP(30);
}

template <int L> struct Loader {
  rsrc_t rx, rq, ro, rv, rk;
  u32 vx[2], vq[2], vo[2], vv[2], ve, vk;
  u32 xso, qso, oso, vso0, vso1, eso, kso[4];
  int hown, h0, kvsh;
  static constexpr int N1 = 4 + (L == 2 ? 10 : 11);
  static constexpr int NK = L == 2 ? 6 : 5;
  static constexpr int N2N = 4 + (L == 0 ? 6 : 5);
  static constexpr int N2E = N2N + (L == 0 ? 0 : 2);
  DI void p1(int ks, LAS char* q) {
    const u32 xs = xso + ks * 128, ws_ = qso + ks * (64 * 8192);
#pragma unroll
    for (int i = 0; i < 4; ++i) { const int p = L + 3 * i; dma(rx, q + p * 1024, vx[p & 1], xs + p * (8 * 1152)); }
#pragma unroll
    for (int i = 0; i < 11; ++i) { const int p = L + 3 * i; if (p < 32) dma(rq, q + 12288 + p * 1024, vq[(p >> 1) & 1], ws_ + (p & 15) * (4 * 8192) + (p >> 4) * 256); }
  }
  DI void pk_(int kt, LAS char* d) {
#pragma unroll
    for (int i = 0; i < 6; ++i) { const int p = (2 - L) + 3 * i; if (p < 16) dma(rk, d + p * 1024, vk ^ ((p & 3) << 6), kso[p >> 2] + kt * 4096 + (p & 3) * 1024); }
  }
  template <bool EXT> DI void p2(int st, LAS char* r) {
    const int h = h0 + ((hown + (st >> 1)) & 7);
    const u32 row = (u32)(h * 128 + (st & 1) * 64) * 1152u;
    const u32 vh = (u32)(h >> kvsh) * (48 * 256) + (st & 1) * 128;
#pragma unroll
    for (int i = 0; i < 4; ++i) { const int p = L + 3 * i; dma(rv, r + 16384 + p * 1024, vv[p & 1], (p < 6 ? vso0 : vso1) + vh + (p % 6) * 2048); }
#pragma unroll
    for (int i = 0; i < 6; ++i) { const int p = L + 3 * i; if (p < 16) dma(ro, r + p * 1024, vo[(p >> 1) & 1], oso + row + p * (4 * 1152)); }
    if (EXT && L > 0) {
#pragma unroll
      for (int i = 0; i < 2; ++i) { const int p = (L - 1) + 2 * i; dma(ro, r + 28672 + p * 1024, ve, eso + row + p * (16 * 1152)); }
    }
  }
};

template <int L> DI void loader(Ctx& c, LAS char* sm) {
  const int lane = lane_id();
  start(c, sm, lane, 4 + L);
  Loader<L> z;
  const int hkv = 32 >> c.kvsh;
  z.rx = mk(c.x, 768 * 1152); z.rq = mk(c.wq, 576 * 8192); z.ro = mk(c.wo, 4096 * 1152);
  z.rv = mk(c.v, (u32)(16 * hkv) * (48 * 256)); z.rk = mk(c.k, (u32)(16 * hkv) * (48 * 256));
  const u32 l3 = lane >> 3, c8 = lane & 7, l4 = lane >> 4, c16 = lane & 15;
  const u32 cx0 = (c8 ^ l4) * 16, cx1 = (c8 ^ (l4 | 4)) * 16, cw0 = (c16 ^ (l4 << 1)) * 16, cw1 = (c16 ^ ((l4 | 4) << 1)) * 16;
  z.vx[0] = l3 * 1152 + cx0; z.vx[1] = l3 * 1152 + cx1;
  z.vq[0] = l4 * 8192 + cw0; z.vq[1] = l4 * 8192 + cw1;
  z.xso = (u32)(c.bp * 96) * 1152u;
  z.hown = 2 * c.j; z.h0 = c.h0; z.kvsh = c.kvsh;
  z.qso = (u32)(c.h0 + z.hown) * 256u;
  z.vo[0] = l4 * 1152 + cw0; z.vo[1] = l4 * 1152 + cw1;
  z.vv[0] = l3 * 256 + cx0; z.vv[1] = l3 * 256 + cx1;
  z.vk = l4 * 256 + (c16 ^ l4) * 16;
  z.ve = (lane >> 2) * 1152 + ((lane & 3) ^ ((lane >> 5) << 1)) * 16;
  z.oso = c.j * 256; z.eso = 1024 + (c.j & 1) * 64;
  z.vso0 = (u32)(2 * c.bp * hkv) * (48 * 256); z.vso1 = z.vso0 + (u32)hkv * (48 * 256);
#pragma unroll
  for (int w = 0; w < 4; ++w) z.kso[w] = ((w >> 1) ? z.vso1 : z.vso0) + (u32)((c.h0 + z.hown + (w & 1)) >> c.kvsh) * (48 * 256);
  STAMP(33); WAITV(5); STAMP(34); BARRIER();
  int slot = 2;
#pragma unroll 1
  for (int s = 2; s < 9; ++s) {
    z.p1(s, sm + slot * QP);
    if (slot == 2) { z.pk_(s / 3, sm + KS); WAITV(Loader<L>::N1 + Loader<L>::NK); slot = 0; } else { WAITV(Loader<L>::N1); ++slot; }
    BARRIER();
  }
#pragma unroll 1
  for (int s = 0; s < 8; ++s) { z.template p2<true>(s, sm + slot * RP); slot = slot == 2 ? 0 : slot + 1; WAITV(Loader<L>::N2E); BARRIER(); }
#pragma unroll 1
  for (int s = 8; s < 16; ++s) { z.template p2<false>(s, sm + slot * RP); slot = slot == 2 ? 0 : slot + 1; WAITV(Loader<L>::N2N); BARRIER(); }
  WAITV(0); BARRIER();
  STAMP(31);
}

struct Pace {
  int g, budget;
  DI void bar() { BARRIER(); ++g; budget = 4; }
  DI void idle(int dead) {
    for (;;) {
      if (vmcnt_now() == 0u) break;
      if (budget > 0) { __builtin_amdgcn_s_sleep(1); --budget; }
      else if (g < dead) bar();
      else break;
    }
    WAITV(0);
  }
};
DI u64 ld_async(const char* base, u32 off) { u64 r; asm volatile("global_load_dwordx2 %0, %1, %2 sc1" : "=v"(r) : "v"(off), "s"(base) : "memory"); return r; }

DI void comm(Ctx& c, LAS char* sm) {
  const int lane = lane_id();
  start(c, sm, lane, 7);
  char* ws = c.ws;
  if (CODEPF) {
    u32 line = ((blockIdx.x >> 3) % 5) * 64 + lane; line = line < CODEPF * 8 ? line : 0u;
    asm volatile("s_mov_b32 m0, %0\n\ts_nop 0\n\tglobal_load_lds_dword %1, %2" :: "s"((u32)(uintptr_t)sm + JUNK), "v"(line * 128), "s"(c.pc & ~255ull) : "memory", "m0");
  }
  const int sg = c.br * 8 + c.bp, me = c.rank * 4 + c.j, m = lane & 15;
  const char* F1 = ws + WS_F1 + sg * 2048;
  const char* R = ws + WS_R + sg * 2048;
  Pace pc;
  __builtin_amdgcn_s_setprio(3);
  STAMP(33); WAITV(5 + (CODEPF ? 1 : 0)); STAMP(34); BARRIER();
  u64 tok = ld_async(F1, m * 128);
  WAITV(1);
  pc.g = 1; pc.bar();
  pc.idle(6); PIN(tok);
  tok += TOKK;
  if (lane < 16) { st_agent((GAS u64*)(R + m * 128 + me * 8), tok); *(LAS u64*)(sm + MISC + m * 8) = tok; }
  if (lane == 0) *(LAS u32*)(sm + MISC + 128) = 0u;
  const u64 mytok = rdlane64(tok, me);
  asm volatile("s_waitcnt lgkmcnt(0)" ::: "memory");
  while (pc.g < 8) pc.bar();
  for (u32 spins = 0; spins < SPIN_CAP; ++spins) {
    u64 r = ld_async(R, me * 128 + m * 8);
    pc.idle(XB + 1); PIN(r);
    if (__all(r == mytok)) break;
  }
  while (pc.g <= XB) pc.bar();
  if (lane == 0) st_agent((GAS u64*)(F1 + me * 128), mytok);
  STAMP(40);
  const rsrc_t rp = mk(ws + WS_P, 256u * 18432u);
  const u32 foff = (u32)(c.rank * 4 + (lane & 3)) * 128;
  const u64 want = __shfl(tok, c.rank * 4 + (lane & 3));
  u32 seen = 0;
#pragma unroll 1
  for (int t = 1; t < 4; ++t) {
    const int pj = (c.j + t) & 3, dead = 13 + 4 * t;
    for (u32 spins = 0; !((seen >> pj) & 1u) && spins < SPIN_CAP; ++spins) {
      u64 r = ld_async(F1, foff);
      do { if (pc.g < dead - 1) pc.bar(); } while (vmcnt_now() != 0u);
      WAITV(0); PIN(r);
      seen = (u32)__ballot(r == want) & 15u;
    }
    STAMP(40 + 2 * t - 1);
    const u32 so = (u32)((c.bp * 4 + pj) * 8 + c.rank * 2 + c.br) * 18432u;
    u32x4 pv[18];
#pragma unroll
    for (int i = 0; i < 18; ++i) asm volatile("buffer_load_dwordx4 %0, %1, %2, %3 offen offset:%4 sc1" : "=v"(pv[i]) : "v"(lane * 16 + (i >> 2) * 4096), "s"(rp), "s"(so), "i"((i & 3) * 1024) : "memory");
    STAMP(47 + 3 * t);
    do { if (pc.g < dead) pc.bar(); } while (vmcnt_now() != 0u || (t == 3 && pc.g <= 14));
    WAITV(0);
    STAMP(48 + 3 * t);
    LAS char* dst = sm + (t == 1 ? P_B : t == 2 ? P_C : P_A) + lane * 16;
#pragma unroll
    for (int i = 0; i < 18; ++i) { PIN(pv[i]); *(LAS u32x4*)(dst + i * 1024) = pv[i]; }
    asm volatile("s_waitcnt lgkmcnt(0)" ::: "memory");
    STAMP(40 + 2 * t);
    if (pc.g <= dead) pc.bar();
  }
  while (pc.g <= 25) pc.bar();
}

template <bool ADV> DI void half1(f32x4 (&Q)[8][3], const u32x4 (&A)[8], const u32x4 (&B)[3], u32x4 (&An)[8], u32x4 (&Bn)[3],
                                  LAS char* (&w)[8], LAS char* (&x)[2], int u, int adv) {
  u32x2 lo[8];
#pragma unroll
  for (int i = 0; i < 24; ++i) {
    Q[i / 3][i % 3] = mfma(A[i / 3], B[i % 3], Q[i / 3][i % 3]);
    constexpr int what[19] = {70, 71, 00, 01, 100, 10, 11, 101, 20, 21, 102, 30, 31, 60, 40, 41, 61, 50, 51};
    if (i < 19) {
      const int k = what[i];
      if (k >= 100) Bn[k - 100] = lds128(x[u] + (k - 100) * 2048);
      else if (k % 10 == 0) lo[k / 10] = trd(w[k / 10] + u * 8192);
      else An[k / 10] = cat(lo[k / 10], trd(w[k / 10] + u * 8192 + 1024));
    } else if (ADV) {
      if (i < 23) { w[2 * (i - 19)] = vadd(w[2 * (i - 19)], adv); w[2 * (i - 19) + 1] = vadd(w[2 * (i - 19) + 1], adv); }
      else { x[0] = vadd(x[0], adv); x[1] = vadd(x[1], adv); }
    }
    SB();
  }
}
struct G2 { u32x4 v[3], w[5]; u32x2 lo; };
struct A2 { LAS char* w[4]; LAS char* e; LAS char* v[2]; };
DI void shift(A2& a, int d) {
#pragma unroll
  for (int i = 0; i < 4; ++i) a.w[i] = vadd(a.w[i], d);
  a.e = vadd(a.e, d); a.v[0] = vadd(a.v[0], d); a.v[1] = vadd(a.v[1], d);
}
template <bool EXT> DI void rd2(int i, G2& f, const A2& a, int off, int u) {
  if (i < 3) f.v[i] = lds128(a.v[u] + off + 16384 + i * 2048);
  else if (i < 11) { const int ct = (i - 3) >> 1; if ((i - 3) & 1) f.w[ct] = cat(f.lo, trd(a.w[ct] + off + u * 8192 + 1024)); else f.lo = trd(a.w[ct] + off + u * 8192); }
  else if (EXT && i == 11) f.lo = trd(a.e + off + 28672 + u * 2048);
  else if (EXT && i == 12) f.w[4] = cat(f.lo, trd(a.e + off + 28672 + u * 2048 + 256));
}
template <bool E, bool FIRST, bool READ, bool PACK, int DBL>
DI void step2(f32x4 (&U)[3][5], const G2& f, G2& fn, const A2& a, int off, int u, u32x4 (&UA)[5], u32x2 (&UC)[5]) {
  const f32x4 z = {0.f, 0.f, 0.f, 0.f};
#pragma unroll
  for (int i = 0; i < (E ? 15 : 12); ++i) {
    const int ct = i / 3, kt = i % 3;
    if (PACK) { const u32x2 t = pk4(U[kt][ct]); if (kt == 0) { UA[ct][0] = t[0]; UA[ct][1] = t[1]; } else if (kt == 1) { UA[ct][2] = t[0]; UA[ct][3] = t[1]; } else UC[ct] = t; SB(); }
    U[kt][ct] = mfma(f.v[kt], f.w[ct], FIRST ? z : U[kt][ct]);
    if (READ) { if (i < DBL) { rd2<E>(2 * i, fn, a, off, u); rd2<E>(2 * i + 1, fn, a, off, u); } else rd2<E>(i + DBL, fn, a, off, u); }
    SB();
  }
}
template <bool PE, bool CE, bool READ, bool FIRST>
DI void finish2(f32x4 (&O)[5][3], f32x4 (&U)[3][5], const u32x4 (&A0)[5], const u32x2 (&C0)[5], G2& f0, G2& f1, const A2& a, int off, LAS char* p, LAS char* pc) {
  constexpr int N = PE ? 15 : 12;
  u32x4 pa0[3], pa1[3], pcc[3], A1[5]; u32x2 C1[5], lo[3];
#pragma unroll
  for (int i = 0; i < N; ++i) {
    U[i % 3][i / 3] = mfma(f1.v[i % 3], f1.w[i / 3], U[i % 3][i / 3]);
    if (i < 3) pa0[i] = lds128(p + i * 1024);
    else if (i < 6) pa1[i - 3] = lds128(p + 4608 + (i - 3) * 1024);
    else if (READ) rd2<CE>(i - 6, f0, a, off, 0);
    SB();
  }
#pragma unroll
  for (int i = 0; i < N; ++i) {
    const int ct = i / 3, n = i % 3;
    O[ct][n] = mfma(A0[ct], pa0[n], FIRST ? (f32x4){0.f, 0.f, 0.f, 0.f} : O[ct][n]);
    const u32x2 t = pk4(U[n][ct]);
    if (n == 0) { A1[ct][0] = t[0]; A1[ct][1] = t[1]; } else if (n == 1) { A1[ct][2] = t[0]; A1[ct][3] = t[1]; } else C1[ct] = t;
    SB();
  }
#pragma unroll
  for (int i = 0; i < N; ++i) {
    O[i / 3][i % 3] = mfma(A1[i / 3], pa1[i % 3], O[i / 3][i % 3]);
    if (i < 3) lo[i] = lds64(pc + i * 512);
    else if (i < 6) pcc[i - 3] = cat(lo[i - 3], lds64(pc + 4608 + (i - 3) * 512));
    else if (READ) rd2<CE>(i + N - 12, f0, a, off, 0);
    SB();
  }
  if (READ && N == 12) rd2<CE>(12, f0, a, off, 0);
#pragma unroll
  for (int i = 0; i < N; ++i) {
    O[i / 3][i % 3] = mfma(cat(C0[i / 3], C1[i / 3]), pcc[i % 3], O[i / 3][i % 3]);
    if (READ) rd2<CE>(i, f1, a, off, 1);
    SB();
  }
  if (READ && N == 12) rd2<CE>(12, f1, a, off, 1);
}
DI float max3(float a, float b, float c) { float r; asm("v_max3_f32 %0, %1, %2, %3" : "=v"(r) : "v"(a), "v"(b), "v"(c)); return r; }
DI float max2(float a, float b) { float r; asm("v_max_f32 %0, %1, %2" : "=v"(r) : "v"(a), "v"(b)); return r; }

DI void tail(Ctx& c, LAS char* sm, int wv, char* ws, char* out, f32x4 (&O)[5][3], bool xown) {
  const int lane = lane_id();
  const int hl = wv & 1, g = lane >> 4, m = lane & 15, b = 2 * c.bp + (wv >> 1);
  const u32 cu = blockIdx.x;
  const int sg = c.br * 8 + c.bp, me = c.rank * 4 + c.j;
  GAS u64* F3 = (GAS u64*)(ws + WS_F3) + sg * 256;
  const rsrc_t rm = mk(ws + WS_M, 1024u * 6144u);
  if (wv != c.rank) {
#pragma unroll
    for (int n = 0; n < 3; ++n)
#pragma unroll
      for (int c2 = 0; c2 < 2; ++c2) __builtin_amdgcn_raw_buffer_store_b128(cat(pk4(O[2 * c2][n]), pk4(O[2 * c2 + 1][n])), rm, lane * 16 + (n * 2 + c2) * 1024, (cu * 4 + wv) * 6144u, 16);
  }
  WAITV(0);
  STAMP(28);
  const u64 tok = *(LAS u64*)(sm + MISC + m * 8);
  {
    u32 old = 0;
    if (lane == 0) old = __hip_atomic_fetch_add((LAS u32*)(sm + MISC + 128), 1u, __ATOMIC_RELAXED, __HIP_MEMORY_SCOPE_WORKGROUP);
    old = __builtin_amdgcn_readfirstlane(old);
    if (old == 3u && lane == me) st_agent(F3 + me * 16, tok);
  }
  if (wv != c.rank) return;
  {
    const bool need = m != me && ((m & 3) == c.j || (xown && (m & 3) == (c.j ^ 2)));
    u32 spins = 0;
    while (!__all(!need || ld_agent(F3 + m * 16) == tok) && ++spins < SPIN_CAP) __builtin_amdgcn_s_sleep(1);
  }
  STAMP(29);
  u32x4 in[3][6];
#pragma unroll
  for (int r = 0; r < 3; ++r) {
    const u32 pr = (u32)((c.rank + 1 + r) & 3), pcu = (cu & ~6u) | (pr << 1);
#pragma unroll
    for (int i = 0; i < 6; ++i) in[r][i] = __builtin_amdgcn_raw_buffer_load_b128(rm, lane * 16 + i * 1024, (pcu * 4 + wv) * 6144u, 16);
  }
  const rsrc_t ro = mk(out, 768 * 2304);
  const u32 orow = (u32)(b * 48 + m) * 2304u + c.br * 1152;
  if (xown) {
    const rsrc_t rxx = mk(ws + WS_X, 1024u * 2048u);
    u32x2 xin[7][3];
#pragma unroll
    for (int r = 0; r < 7; ++r) {
      const u32 pr = (u32)((c.rank + 1 + r) & 3), pcu = ((cu & ~6u) | (pr << 1)) ^ (r >= 3 ? 16u : 0u);
#pragma unroll
      for (int n = 0; n < 3; ++n) xin[r][n] = __builtin_amdgcn_raw_buffer_load_b64(rxx, lane * 8 + n * 512, (pcu * 4 + wv) * 2048u, 16);
    }
#pragma unroll
    for (int n = 0; n < 3; ++n) {
      f32x4 a = O[4][n];
#pragma unroll
      for (int r = 0; r < 7; ++r) a += (f32x4){bflo(xin[r][n][0]), bfhi(xin[r][n][0]), bflo(xin[r][n][1]), bfhi(xin[r][n][1])};
      __builtin_amdgcn_raw_buffer_store_b64(pk4(a), ro, orow + n * (16 * 2304) + (512 + 32 * (c.j & 1) + 16 * hl + 4 * g) * 2, 0, 0);
    }
  }
#pragma unroll
  for (int n = 0; n < 3; ++n)
#pragma unroll
    for (int c2 = 0; c2 < 2; ++c2) {
      f32x4 a = O[2 * c2][n], d = O[2 * c2 + 1][n];
#pragma unroll
      for (int r = 0; r < 3; ++r) {
        const u32x4 v = in[r][n * 2 + c2];
        a += (f32x4){bflo(v[0]), bfhi(v[0]), bflo(v[1]), bfhi(v[1])};
        d += (f32x4){bflo(v[2]), bfhi(v[2]), bflo(v[3]), bfhi(v[3])};
      }
      const u32x2 pa = pk4(a), pd = pk4(d);
      auto s0 = __builtin_amdgcn_permlane16_swap(pa[0], pd[0], false, false);
      auto s1 = __builtin_amdgcn_permlane16_swap(pa[1], pd[1], false, false);
      __builtin_amdgcn_raw_buffer_store_b128((u32x4){s0[0], s1[0], s0[1], s1[1]}, ro,
          orow + n * (16 * 2304) + (128 * c.j + 64 * hl + 32 * c2 + 16 * (g & 1) + 8 * (g >> 1)) * 2, 0, 0);
    }
  STAMP(32);
}

DI void compute(Ctx& c, LAS char* sm, int wv) {
  start(c, sm, lane_id(), wv);
  char* ws = c.ws;
  const int bl = wv >> 1, hl = wv & 1;
  const u32 cu = blockIdx.x;
  int lane = lane_id(), g = lane >> 4, m = lane & 15, q = m >> 2, p = lane & 3;
  u32 wbase = (8 * g + q) * 256 + 32 * (q | ((g & 1) << 2)) + 8 * p;
  u32 xbase = (bl * 48 + m) * 128 + ((g ^ (m >> 1)) * 16);
  u32x4 Kf[3][4];
  f32x4 Q[8][3];
  u32x4 A1[8], B1[3];
  LAS char* aw[8]; LAS char* ax[2];
#pragma unroll
  for (int T = 0; T < 8; ++T) { A1[T] = (u32x4){0, 0, 0, 0}; PIN(A1[T]);
    aw[T] = vadd(sm + (wbase ^ (T << 5)), 12288 + hl * 16384);
#pragma unroll
    for (int n = 0; n < 3; ++n) { Q[T][n] = (f32x4){0.f, 0.f, 0.f, 0.f}; PIN(Q[T][n]); } }
#pragma unroll
  for (int n = 0; n < 3; ++n) { B1[n] = (u32x4){0, 0, 0, 0}; PIN(B1[n]); }
  ax[0] = vadd(sm + xbase, 0); ax[1] = vadd(sm + (xbase ^ 64), 0);
  SB();
  STAMP(33); WAITV(5); STAMP(34); BARRIER();
  STAMP(0);
#pragma unroll
  for (int kt = 0; kt < 3; ++kt) {
#pragma unroll 1
    for (int s = 0; s < 3; ++s) {
      if (s == 2) {
        const u32 l = lane_id(), kb = KS + wv * 4096 + (l & 15) * 256 + ((l >> 5) ^ (l & 15)) * 16 + ((l >> 1) & 8);
#pragma unroll
        for (int i = 0; i < 4; ++i) Kf[kt][i] = cat(lds64(sm + (kb ^ (i << 6))), lds64(sm + (kb ^ (i << 6) ^ 32)));
      }
      u32x4 A0[8], B0[3];
      half1<false>(Q, A1, B1, A0, B0, aw, ax, 0, 0);
      half1<true>(Q, A0, B0, A1, B1, aw, ax, 1, s == 2 ? -2 * QP : QP);
      if (kt == 0 && s == 0) WAITV(0);
      LBARRIER();
      STAMP(1 + 3 * kt + s);
    }
  }
  SB();
  lane = lane_id(); g = lane >> 4; m = lane & 15; q = m >> 2; p = lane & 3;
  wbase = (8 * g + q) * 256 + 32 * (q | ((g & 1) << 2)) + 8 * p;
  xbase = (bl * 48 + m) * 128 + ((g ^ (m >> 1)) * 16);
  A2 a;
#pragma unroll
  for (int ct = 0; ct < 4; ++ct) a.w[ct] = sm + (wbase ^ ((4 * hl + ct) << 5));
  a.e = sm + (8 * g + q) * 64 + 32 * (hl ^ (g & 1)) + 8 * p;
  a.v[0] = sm + xbase; a.v[1] = sm + (xbase ^ 64);
  G2 f0, f1;
  f32x4 O[5][3], U[3][5];
  {
    u32x4 qf[3][4];
    f32x4 S[3][3];
#pragma unroll
    for (int i = 0; i < 24; ++i) {
      Q[i & 7][i >> 3] = mfma(A1[i & 7], B1[i >> 3], Q[i & 7][i >> 3]);
      if (i >= 8) { const u32x2 t = pk4(Q[i & 7][(i >> 3) - 1]); qf[(i >> 3) - 1][(i & 7) >> 1][2 * (i & 1)] = t[0]; qf[(i >> 3) - 1][(i & 7) >> 1][2 * (i & 1) + 1] = t[1]; }
      SB();
    }
    STAMP(36);
#pragma unroll
    for (int i = 0; i < 36; ++i) {
      const int n = i / 12, s = (i % 12) / 3, kt = i % 3;
      S[n][kt] = mfma(Kf[kt][s], qf[n][s], s == 0 ? (f32x4){0.f, 0.f, 0.f, 0.f} : S[n][kt]);
      if (i >= 2 && i < 10) { const u32x2 t = pk4(Q[i - 2][2]); qf[2][(i - 2) >> 1][2 * (i & 1)] = t[0]; qf[2][(i - 2) >> 1][2 * (i & 1) + 1] = t[1]; }
      if (i >= 10 && i < 23) rd2<true>(i - 10, f0, a, 0, 0);
      if (i >= 23) rd2<true>(i - 23, f1, a, 0, 1);
      SB();
    }
    STAMP(37);
    const rsrc_t rp = mk(ws + WS_P, 256u * 18432u);
    const u32 pso = cu * 18432u + wv * 4608;
    LAS char* pl = vadd(sm + lane * 16, P_A + wv * 4608);
    LAS char* pl2 = vadd(sm + lane * 8, P_A + wv * 4608 + 3072);
    float mx[3], sum[3];
    int um = 0;
#define UM() do { if (um < 15) { U[um % 3][um / 3] = mfma(f0.v[um % 3], f0.w[um / 3], (f32x4){0.f, 0.f, 0.f, 0.f}); ++um; } SB(); } while (0)
#pragma unroll
    for (int n = 0; n < 3; ++n)
      mx[n] = max3(max3(S[n][0][0], S[n][0][1], S[n][0][2]), max3(S[n][0][3], S[n][1][0], S[n][1][1]),
                   max3(max3(S[n][1][2], S[n][1][3], S[n][2][0]), max3(S[n][2][1], S[n][2][2], S[n][2][3]), S[n][0][0]));
#pragma unroll
    for (int n = 0; n < 3; ++n) { auto r = __builtin_amdgcn_permlane16_swap(__float_as_uint(mx[n]), __float_as_uint(mx[n]), false, false); mx[n] = max2(__uint_as_float(r[0]), __uint_as_float(r[1])); }
#pragma unroll
    for (int n = 0; n < 3; ++n) { auto r = __builtin_amdgcn_permlane32_swap(__float_as_uint(mx[n]), __float_as_uint(mx[n]), false, false); mx[n] = max2(__uint_as_float(r[0]), __uint_as_float(r[1])) * -0.12751743344863963f; }
    SB();
#pragma unroll
    for (int n = 0; n < 3; ++n)
#pragma unroll
      for (int kt = 0; kt < 3; ++kt) {
#pragma unroll
        for (int i = 0; i < 4; ++i) S[n][kt][i] = __builtin_amdgcn_exp2f(__builtin_fmaf(S[n][kt][i], 0.12751743344863963f, mx[n]));
        UM();
      }
    STAMP(38);
#pragma unroll
    for (int n = 0; n < 3; ++n) { const f32x4 s4 = S[n][0] + S[n][1] + S[n][2]; sum[n] = (s4[0] + s4[1]) + (s4[2] + s4[3]); UM(); }
#pragma unroll
    for (int n = 0; n < 3; ++n) { auto r = __builtin_amdgcn_permlane16_swap(__float_as_uint(sum[n]), __float_as_uint(sum[n]), false, false); sum[n] = __uint_as_float(r[0]) + __uint_as_float(r[1]); }
#pragma unroll
    for (int n = 0; n < 3; ++n) { auto r = __builtin_amdgcn_permlane32_swap(__float_as_uint(sum[n]), __float_as_uint(sum[n]), false, false); sum[n] = __builtin_amdgcn_rcpf(__uint_as_float(r[0]) + __uint_as_float(r[1])); }
    SB();
    STAMP(39);
#pragma unroll
    for (int n = 0; n < 3; ++n) {
      const u32x4 pa = cat(pk4(S[n][0] * sum[n]), pk4(S[n][1] * sum[n]));
      const u32x2 pc = pk4(S[n][2] * sum[n]);
      __builtin_amdgcn_raw_buffer_store_b128(pa, rp, lane * 16 + n * 1024, pso, 16);
      __builtin_amdgcn_raw_buffer_store_b64(pc, rp, lane * 8 + 3072 + n * 512, pso, 16);
      *(LAS u32x4*)(pl + n * 1024) = pa;
      *(LAS u32x2*)(pl2 + n * 512) = pc;
      UM();
    }
  }
  STAMP(26);
  SB();
  LAS char* pp = sm + bl * 9216 + lane * 16;
  LAS char* pq = sm + bl * 9216 + lane * 8 + 3072;
  u32x4 UA[5]; u32x2 UC[5];
  const bool xown = (wv == c.rank) && ((c.j >> 1) == (c.rank & 1));
#define ENDI(gi) do { if ((gi) % 3 == 0) shift(a, RP); if ((gi) % 3 == 2) shift(a, -RP); if ((gi) + 1 == XB) WAITV(0); LBARRIER(); STAMP(1 + (gi)); SB(); } while (0)
#define SLOT(gi) ((gi) % 3 == 2 ? RP : 0)
#define BODY(t, PE, CE) do { \
    constexpr int g0 = 9 + 4 * (t); \
    if ((t) > 0) { \
      constexpr int pa_ = (t) == 1 ? P_A : (t) == 2 ? P_B : P_C; \
      finish2<PE, CE, true, (t) == 1>(O, U, UA, UC, f0, f1, a, SLOT(g0), vadd(pp, pa_), vadd(pq, pa_)); \
      if ((t) == 2 && !xown) { const rsrc_t rxx = mk(ws + WS_X, 1024u * 2048u); \
        _Pragma("unroll") for (int n = 0; n < 3; ++n) __builtin_amdgcn_raw_buffer_store_b64(pk4(O[4][n]), rxx, lane * 8 + n * 512, (cu * 4 + wv) * 2048u, 16); } \
      step2<CE, true, false, false, 0>(U, f0, f1, a, 0, 0, UA, UC); } \
    ENDI(g0); \
    step2<CE, false, true, false, 0>(U, f1, f0, a, SLOT(g0 + 1), 0, UA, UC); \
    step2<CE, false, true, false, 3>(U, f0, f1, a, SLOT(g0 + 1), 1, UA, UC); ENDI(g0 + 1); \
    step2<CE, false, true, false, 0>(U, f1, f0, a, SLOT(g0 + 2), 0, UA, UC); \
    step2<CE, true, true, true, 3>(U, f0, f1, a, SLOT(g0 + 2), 1, UA, UC); ENDI(g0 + 2); \
    step2<CE, false, true, false, 0>(U, f1, f0, a, SLOT(g0 + 3), 0, UA, UC); \
    step2<CE, false, true, false, 3>(U, f0, f1, a, SLOT(g0 + 3), 1, UA, UC); ENDI(g0 + 3); \
  } while (0)
  BODY(0, true, true);
  BODY(1, true, true);
  BODY(2, true, false);
  BODY(3, false, false);
  finish2<false, false, false, false>(O, U, UA, UC, f0, f1, a, 0, vadd(pp, P_A), vadd(pq, P_A));
  STAMP(27);
  tail(c, sm, wv, ws, c.out, O, xown);
}

__global__ __launch_bounds__(512, 2) void fused_attn_block(const char* xA, const char* kA, const char* vA, const char* wqA, const char* woA,
                                                          const char* xB, const char* kB, const char* vB, const char* wqB, const char* woB,
                                                          char* out, char* ws) {
  __shared__ __attribute__((aligned(256))) char smem[LDS_BYTES];
  LAS char* sm = (LAS char*)smem;
  const int wv = __builtin_amdgcn_readfirstlane(threadIdx.x >> 6);
  const u64 tent = __builtin_amdgcn_s_memrealtime();
  Ctx c;
  c.stamps = 0; c.ta = 0; c.tb = 0;
  STAMP(63);
  const int id = blockIdx.x;
  c.br = id & 1; c.rank = (id >> 1) & 3; c.j = (id >> 3) & 3; c.bp = id >> 5;
  c.h0 = c.rank * 8; c.kvsh = 1 - c.br;
  {
    const u64 ka = (u64)(uintptr_t)__builtin_amdgcn_kernarg_segment_ptr();
    asm volatile("s_load_dwordx2 %0, %8, 0x0\n\ts_load_dwordx2 %1, %8, 0x18\n\ts_load_dwordx2 %2, %8, 0x8\n\ts_load_dwordx2 %3, %8, 0x10\n\t"
                 "s_load_dwordx2 %4, %8, 0x20\n\ts_load_dwordx2 %5, %9, 0x50\n\ts_load_dwordx2 %6, %9, 0x58\n\ts_getpc_b64 %7"
                 : "=&s"(c.a[0]), "=&s"(c.a[1]), "=&s"(c.a[2]), "=&s"(c.a[3]), "=&s"(c.a[4]), "=&s"(c.a[5]), "=&s"(c.a[6]), "=&s"(c.pc)
                 : "s"(ka + c.br * 40), "s"(ka) : "memory");
  }
  if (wv == 4) loader<0>(c, sm);
  else if (wv == 5) loader<1>(c, sm);
  else if (wv == 6) loader<2>(c, sm);
  else if (wv == 7) comm(c, sm);
  else if (ABL_NOCOMP) { start(c, sm, lane_id(), wv); STAMP(33); WAITV(5); STAMP(34); BARRIER(); STAMP(0); WAITV(0); for (int g = 1; g < 26; ++g) { BARRIER(); STAMP(g); } }
  else compute(c, sm, wv);
}

__global__ void pre_k(u64* p) { st_agent((GAS u64*)p, (u64)__builtin_amdgcn_s_memrealtime()); }
__global__ void post_k(u64* p) { const u64 t0 = __builtin_amdgcn_s_memrealtime(); const u64 d = (t0 - ld_agent((GAS u64*)p)) * 20u; while (__builtin_amdgcn_s_memrealtime() - t0 < d) __builtin_amdgcn_s_sleep(4); }
extern "C" void kernel_launch(void* const* d_in, const int* in_sizes, int n_in, void* d_out, int out_size, void* d_ws, size_t ws_size,
                              hipStream_t stream) {
  const char* const* in = (const char* const*)d_in;
  pre_k<<<1, 64, 0, stream>>>((u64*)((char*)d_ws + (32u << 20)));
  fused_attn_block<<<256, 512, 0, stream>>>(in[0], in[1], in[2], in[3], in[4], in[5], in[6], in[7], in[8], in[9], (char*)d_out, (char*)d_ws);
}
```

```cpp
#define AMP_A 4
#define AMP_B 5
#include <hip/hip_runtime.h>
#include <stdint.h>

typedef unsigned u32;
typedef unsigned long long u64;
typedef u32 u32x4 __attribute__((ext_vector_type(4)));
typedef u32 u32x2 __attribute__((ext_vector_type(2)));
typedef u32 u32x8 __attribute__((ext_vector_type(8)));
typedef float f32x4 __attribute__((ext_vector_type(4)));
typedef float f32x2 __attribute__((ext_vector_type(2)));
typedef short s16x4 __attribute__((ext_vector_type(4)));
typedef __bf16 bf16x8 __attribute__((ext_vector_type(8)));
typedef __bf16 bf16x2 __attribute__((ext_vector_type(2)));
#define LAS __attribute__((address_space(3)))
#define GAS __attribute__((address_space(1)))
#define DI __device__ __forceinline__
typedef __amdgpu_buffer_rsrc_t rsrc_t;

#define DIAG 0
#define CODEPF 35
#define XB 11
#define ABL_NOLOAD 0
#define ABL_NOCOMP 0

constexpr int QP = 45056;
constexpr int RP = 32768;
constexpr int KS = 135168;
constexpr int P_A = 98304, P_B = 116736, P_C = 135168;
constexpr int MISC = 153600;
constexpr int JUNK = 153856;
constexpr int LDS_BYTES = 154112;
constexpr u32 WS_F1 = 0;
constexpr u32 WS_F3 = 32768;
constexpr u32 WS_R = 65536;
constexpr u32 WS_P = 131072;
constexpr u32 WS_M = WS_P + 256u * 18432u;
constexpr u32 WS_X = WS_M + 1024u * 6144u;
constexpr u32 WS_DIAG = 16u << 20;
constexpr u64 TOKK = 0x9E3779B97F4A7C15ull;
constexpr u32 SPIN_CAP = 1u << 20;

DI rsrc_t mk(const void* p, u32 bytes) { return __builtin_amdgcn_make_buffer_rsrc((void*)p, 0, bytes, 0x00020000); }
DI void dma(rsrc_t r, LAS char* dst, u32 voff, u32 soff) { if (ABL_NOLOAD) return; __builtin_amdgcn_raw_ptr_buffer_load_lds(r, (LAS void*)dst, 16, voff, soff, 0, 0); }
DI void dma_asm(rsrc_t r, u32 dst, u32 voff, u32 soff) {
  if (ABL_NOLOAD) return;
  asm volatile("s_mov_b32 m0, %0\n\ts_nop 0\n\tbuffer_load_dwordx4 %1, %2, %3 offen lds" :: "s"(dst), "v"(voff), "s"(r), "s"(soff) : "memory", "m0");
}
#define WAITV(n) asm volatile("s_waitcnt vmcnt(%0)" ::"i"(n) : "memory")
#define BARRIER() asm volatile("s_barrier" ::: "memory")
#define LBARRIER() asm volatile("s_waitcnt lgkmcnt(0)\n\ts_barrier" ::: "memory")
#define SB() __builtin_amdgcn_sched_barrier(0)
#define PIN(x) asm volatile("" : "+v"(x))
DI int lane_id() { int l = __builtin_amdgcn_mbcnt_hi(~0u, __builtin_amdgcn_mbcnt_lo(~0u, 0u)); PIN(l); return l; }
DI u32 vmcnt_now() { u32 v; asm volatile("s_getreg_b32 %0, hwreg(HW_REG_IB_STS)" : "=s"(v) :: "memory"); return v & 0x00C0000Fu; }
DI f32x4 mfma(u32x4 a, u32x4 b, f32x4 c) { return __builtin_amdgcn_mfma_f32_16x16x32_bf16(__builtin_bit_cast(bf16x8, a), __builtin_bit_cast(bf16x8, b), c, 0, 0, 0); }
DI u32 pk(float a, float b) { f32x2 f = {a, b}; return __builtin_bit_cast(u32, __builtin_convertvector(f, bf16x2)); }
DI u32x2 pk4(f32x4 v) { u32x2 r = {pk(v[0], v[1]), pk(v[2], v[3])}; return r; }
DI u32x4 cat(u32x2 a, u32x2 b) { u32x4 r = {a[0], a[1], b[0], b[1]}; return r; }
DI u32x2 trd(LAS char* p) { return __builtin_bit_cast(u32x2, __builtin_amdgcn_ds_read_tr16_b64_v4i16((LAS s16x4*)p)); }
DI u32x4 lds128(LAS char* p) { return *(LAS u32x4*)p; }
DI u32x2 lds64(LAS char* p) { return *(LAS u32x2*)p; }
DI float bflo(u32 x) { return __builtin_bit_cast(float, x << 16); }
DI float bfhi(u32 x) { return __builtin_bit_cast(float, x & 0xffff0000u); }
DI u64 ld_agent(GAS u64* p) { return __hip_atomic_load(p, __ATOMIC_RELAXED, __HIP_MEMORY_SCOPE_AGENT); }
DI void st_agent(GAS u64* p, u64 v) { __hip_atomic_store(p, v, __ATOMIC_RELAXED, __HIP_MEMORY_SCOPE_AGENT); }
DI u64 rdlane64(u64 v, int l) { return ((u64)(u32)__builtin_amdgcn_readlane((int)(v >> 32), l) << 32) | (u32)__builtin_amdgcn_readlane((int)v, l); }
DI LAS char* vadd(LAS char* p, int d) { u32 r; asm volatile("v_add_u32 %0, %1, %2" : "=v"(r) : "s"(d), "v"((u32)(uintptr_t)p)); return (LAS char*)(uintptr_t)r; }
DI int wlane(int val, int idx, int old) { asm volatile("s_mov_b32 m0, %2\n\ts_nop 0\n\tv_writelane_b32 %0, %1, m0" : "+v"(old) : "s"(val), "s"(idx) : "m0"); return old; }
#define STAMP(i) do { const int i_ = (i); if (i_ == AMP_A || i_ == AMP_B) { u64 t_; asm volatile("s_memtime %0\n\ts_waitcnt lgkmcnt(0)" : "=s"(t_) :: "memory"); if (i_ == AMP_A) c.ta = (u32)t_; else c.tb = (u32)t_; } } while (0)

struct Ctx {
  const char *x, *k, *v, *wq, *wo;
  u64 a[7], pc;
  char *out, *ws;
  int stamps; u32 ta, tb;
  int br, rank, bp, j, h0, kvsh;
};

DI void start(Ctx& c, LAS char* sm, int lane, int wv) {
  asm volatile("s_waitcnt lgkmcnt(0)\n\ts_mov_b64 %0, %7\n\ts_mov_b64 %1, %8\n\ts_mov_b64 %2, %9\n\ts_mov_b64 %3, %10\n\ts_mov_b64 %4, %11\n\ts_mov_b64 %5, %12\n\ts_mov_b64 %6, %13"
               : "=&s"(c.x), "=&s"(c.wq), "=&s"(c.k), "=&s"(c.v), "=&s"(c.wo), "=&s"(c.out), "=&s"(c.ws)
               : "s"(c.a[0]), "s"(c.a[1]), "s"(c.a[2]), "s"(c.a[3]), "s"(c.a[4]), "s"(c.a[5]), "s"(c.a[6]) : "memory");
  const rsrc_t rx = mk(c.x, 768 * 1152), rq = mk(c.wq, 576 * 8192);
  const u32 l4 = lane >> 4;
  const u32 vx = (lane >> 3) * 1152 + ((lane & 7) ^ (l4 | ((wv & 1) << 2))) * 16;
  const u32 vq = l4 * 8192 + ((lane & 15) ^ ((l4 | ((wv & 2) << 1)) << 1)) * 16;
  const u32 xs = (u32)(c.bp * 96 + wv * 8) * 1152u, qs = (u32)(c.h0 + 2 * c.j) * 256u + wv * (4 * 8192);
  const u32 d = (u32)(uintptr_t)sm + wv * 1024;
  dma_asm(rx, d, vx, xs);
  dma_asm(rx, (u32)(uintptr_t)sm + (wv >> 2) * QP + (8 + (wv & 3)) * 1024, vx, (u32)(c.bp * 96 + 64 + (wv & 3) * 8) * 1152u + (wv >> 2) * 128);
#pragma unroll
  for (int i = 0; i < 4; ++i) dma_asm(rq, d + 12288 + i * 8192, vq, qs + (i & 1) * (32 * 8192) + (i >> 1) * 256);
  dma_asm(rx, d + QP, vx, xs + 128);
#pragma unroll
  for (int i = 0; i < 4; ++i) dma_asm(rq, d + QP + 12288 + i * 8192, vq, qs + 64 * 8192 + (i & 1) * (32 * 8192) + (i >> 1) * 256);
  STAMP(30);
}

template <int L> struct Loader {
  rsrc_t rx, rq, ro, rv, rk;
  u32 vx[2], vq[2], vo[2], vv[2], ve, vk;
  u32 xso, qso, oso, vso0, vso1, eso, kso[4];
  int hown, h0, kvsh;
  static constexpr int N1 = 4 + (L == 2 ? 10 : 11);
  static constexpr int NK = L == 2 ? 6 : 5;
  static constexpr int N2N = 4 + (L == 0 ? 6 : 5);
  static constexpr int N2E = N2N + (L == 0 ? 0 : 2);
  DI void p1(int ks, LAS char* q) {
    const u32 xs = xso + ks * 128, ws_ = qso + ks * (64 * 8192);
#pragma unroll
    for (int i = 0; i < 4; ++i) { const int p = L + 3 * i; dma(rx, q + p * 1024, vx[p & 1], xs + p * (8 * 1152)); }
#pragma unroll
    for (int i = 0; i < 11; ++i) { const int p = L + 3 * i; if (p < 32) dma(rq, q + 12288 + p * 1024, vq[(p >> 1) & 1], ws_ + (p & 15) * (4 * 8192) + (p >> 4) * 256); }
  }
  DI void pk_(int kt, LAS char* d) {
#pragma unroll
    for (int i = 0; i < 6; ++i) { const int p = (2 - L) + 3 * i; if (p < 16) dma(rk, d + p * 1024, vk ^ ((p & 3) << 6), kso[p >> 2] + kt * 4096 + (p & 3) * 1024); }
  }
  template <bool EXT> DI void p2(int st, LAS char* r) {
    const int h = h0 + ((hown + (st >> 1)) & 7);
    const u32 row = (u32)(h * 128 + (st & 1) * 64) * 1152u;
    const u32 vh = (u32)(h >> kvsh) * (48 * 256) + (st & 1) * 128;
#pragma unroll
    for (int i = 0; i < 4; ++i) { const int p = L + 3 * i; dma(rv, r + 16384 + p * 1024, vv[p & 1], (p < 6 ? vso0 : vso1) + vh + (p % 6) * 2048); }
#pragma unroll
    for (int i = 0; i < 6; ++i) { const int p = L + 3 * i; if (p < 16) dma(ro, r + p * 1024, vo[(p >> 1) & 1], oso + row + p * (4 * 1152)); }
    if (EXT && L > 0) {
#pragma unroll
      for (int i = 0; i < 2; ++i) { const int p = (L - 1) + 2 * i; dma(ro, r + 28672 + p * 1024, ve, eso + row + p * (16 * 1152)); }
    }
  }
};

template <int L> DI void loader(Ctx& c, LAS char* sm) {
  const int lane = lane_id();
  start(c, sm, lane, 4 + L);
  Loader<L> z;
  const int hkv = 32 >> c.kvsh;
  z.rx = mk(c.x, 768 * 1152); z.rq = mk(c.wq, 576 * 8192); z.ro = mk(c.wo, 4096 * 1152);
  z.rv = mk(c.v, (u32)(16 * hkv) * (48 * 256)); z.rk = mk(c.k, (u32)(16 * hkv) * (48 * 256));
  const u32 l3 = lane >> 3, c8 = lane & 7, l4 = lane >> 4, c16 = lane & 15;
  const u32 cx0 = (c8 ^ l4) * 16, cx1 = (c8 ^ (l4 | 4)) * 16, cw0 = (c16 ^ (l4 << 1)) * 16, cw1 = (c16 ^ ((l4 | 4) << 1)) * 16;
  z.vx[0] = l3 * 1152 + cx0; z.vx[1] = l3 * 1152 + cx1;
  z.vq[0] = l4 * 8192 + cw0; z.vq[1] = l4 * 8192 + cw1;
  z.xso = (u32)(c.bp * 96) * 1152u;
  z.hown = 2 * c.j; z.h0 = c.h0; z.kvsh = c.kvsh;
  z.qso = (u32)(c.h0 + z.hown) * 256u;
  z.vo[0] = l4 * 1152 + cw0; z.vo[1] = l4 * 1152 + cw1;
  z.vv[0] = l3 * 256 + cx0; z.vv[1] = l3 * 256 + cx1;
  z.vk = l4 * 256 + (c16 ^ l4) * 16;
  z.ve = (lane >> 2) * 1152 + ((lane & 3) ^ ((lane >> 5) << 1)) * 16;
  z.oso = c.j * 256; z.eso = 1024 + (c.j & 1) * 64;
  z.vso0 = (u32)(2 * c.bp * hkv) * (48 * 256); z.vso1 = z.vso0 + (u32)hkv * (48 * 256);
#pragma unroll
  for (int w = 0; w < 4; ++w) z.kso[w] = ((w >> 1) ? z.vso1 : z.vso0) + (u32)((c.h0 + z.hown + (w & 1)) >> c.kvsh) * (48 * 256);
  STAMP(33); WAITV(5); STAMP(34); BARRIER();
  int slot = 2;
#pragma unroll 1
  for (int s = 2; s < 9; ++s) {
    z.p1(s, sm + slot * QP);
    if (slot == 2) { z.pk_(s / 3, sm + KS); WAITV(Loader<L>::N1 + Loader<L>::NK); slot = 0; } else { WAITV(Loader<L>::N1); ++slot; }
    BARRIER();
  }
#pragma unroll 1
  for (int s = 0; s < 8; ++s) { z.template p2<true>(s, sm + slot * RP); slot = slot == 2 ? 0 : slot + 1; WAITV(Loader<L>::N2E); BARRIER(); }
#pragma unroll 1
  for (int s = 8; s < 16; ++s) { z.template p2<false>(s, sm + slot * RP); slot = slot == 2 ? 0 : slot + 1; WAITV(Loader<L>::N2N); BARRIER(); }
  WAITV(0); BARRIER();
  STAMP(31);
}

struct Pace {
  int g, budget;
  DI void bar() { BARRIER(); ++g; budget = 4; }
  DI void idle(int dead) {
    for (;;) {
      if (vmcnt_now() == 0u) break;
      if (budget > 0) { __builtin_amdgcn_s_sleep(1); --budget; }
      else if (g < dead) bar();
      else break;
    }
    WAITV(0);
  }
};
DI u64 ld_async(const char* base, u32 off) { u64 r; asm volatile("global_load_dwordx2 %0, %1, %2 sc1" : "=v"(r) : "v"(off), "s"(base) : "memory"); return r; }

DI void comm(Ctx& c, LAS char* sm) {
  const int lane = lane_id();
  start(c, sm, lane, 7);
  char* ws = c.ws;
  if (CODEPF) {
    u32 line = ((blockIdx.x >> 3) % 5) * 64 + lane; line = line < CODEPF * 8 ? line : 0u;
    asm volatile("s_mov_b32 m0, %0\n\ts_nop 0\n\tglobal_load_lds_dword %1, %2" :: "s"((u32)(uintptr_t)sm + JUNK), "v"(line * 128), "s"(c.pc & ~255ull) : "memory", "m0");
  }
  const int sg = c.br * 8 + c.bp, me = c.rank * 4 + c.j, m = lane & 15;
  const char* F1 = ws + WS_F1 + sg * 2048;
  const char* R = ws + WS_R + sg * 2048;
  Pace pc;
  __builtin_amdgcn_s_setprio(3);
  STAMP(33); WAITV(5 + (CODEPF ? 1 : 0)); STAMP(34); BARRIER();
  u64 tok = ld_async(F1, m * 128);
  WAITV(1);
  pc.g = 1; pc.bar();
  pc.idle(6); PIN(tok);
  tok += TOKK;
  if (lane < 16) { st_agent((GAS u64*)(R + m * 128 + me * 8), tok); *(LAS u64*)(sm + MISC + m * 8) = tok; }
  if (lane == 0) *(LAS u32*)(sm + MISC + 128) = 0u;
  const u64 mytok = rdlane64(tok, me);
  asm volatile("s_waitcnt lgkmcnt(0)" ::: "memory");
  while (pc.g < 8) pc.bar();
  for (u32 spins = 0; spins < SPIN_CAP; ++spins) {
    u64 r = ld_async(R, me * 128 + m * 8);
    pc.idle(XB + 1); PIN(r);
    if (__all(r == mytok)) break;
  }
  while (pc.g <= XB) pc.bar();
  if (lane == 0) st_agent((GAS u64*)(F1 + me * 128), mytok);
  STAMP(40);
  const rsrc_t rp = mk(ws + WS_P, 256u * 18432u);
  const u32 foff = (u32)(c.rank * 4 + (lane & 3)) * 128;
  const u64 want = __shfl(tok, c.rank * 4 + (lane & 3));
  u32 seen = 0;
#pragma unroll 1
  for (int t = 1; t < 4; ++t) {
    const int pj = (c.j + t) & 3, dead = 13 + 4 * t;
    for (u32 spins = 0; !((seen >> pj) & 1u) && spins < SPIN_CAP; ++spins) {
      u64 r = ld_async(F1, foff);
      do { if (pc.g < dead - 1) pc.bar(); } while (vmcnt_now() != 0u);
      WAITV(0); PIN(r);
      seen = (u32)__ballot(r == want) & 15u;
    }
    STAMP(40 + 2 * t - 1);
    const u32 so = (u32)((c.bp * 4 + pj) * 8 + c.rank * 2 + c.br) * 18432u;
    u32x4 pv[18];
#pragma unroll
    for (int i = 0; i < 18; ++i) asm volatile("buffer_load_dwordx4 %0, %1, %2, %3 offen offset:%4 sc1" : "=v"(pv[i]) : "v"(lane * 16 + (i >> 2) * 4096), "s"(rp), "s"(so), "i"((i & 3) * 1024) : "memory");
    STAMP(47 + 3 * t);
    do { if (pc.g < dead) pc.bar(); } while (vmcnt_now() != 0u || (t == 3 && pc.g <= 14));
    WAITV(0);
    STAMP(48 + 3 * t);
    LAS char* dst = sm + (t == 1 ? P_B : t == 2 ? P_C : P_A) + lane * 16;
#pragma unroll
    for (int i = 0; i < 18; ++i) { PIN(pv[i]); *(LAS u32x4*)(dst + i * 1024) = pv[i]; }
    asm volatile("s_waitcnt lgkmcnt(0)" ::: "memory");
    STAMP(40 + 2 * t);
    if (pc.g <= dead) pc.bar();
  }
  while (pc.g <= 25) pc.bar();
}

template <bool ADV> DI void half1(f32x4 (&Q)[8][3], const u32x4 (&A)[8], const u32x4 (&B)[3], u32x4 (&An)[8], u32x4 (&Bn)[3],
                                  LAS char* (&w)[8], LAS char* (&x)[2], int u, int adv) {
  u32x2 lo[8];
#pragma unroll
  for (int i = 0; i < 24; ++i) {
    Q[i / 3][i % 3] = mfma(A[i / 3], B[i % 3], Q[i / 3][i % 3]);
    constexpr int what[19] = {70, 71, 00, 01, 100, 10, 11, 101, 20, 21, 102, 30, 31, 60, 40, 41, 61, 50, 51};
    if (i < 19) {
      const int k = what[i];
      if (k >= 100) Bn[k - 100] = lds128(x[u] + (k - 100) * 2048);
      else if (k % 10 == 0) lo[k / 10] = trd(w[k / 10] + u * 8192);
      else An[k / 10] = cat(lo[k / 10], trd(w[k / 10] + u * 8192 + 1024));
    } else if (ADV) {
      if (i < 23) { w[2 * (i - 19)] = vadd(w[2 * (i - 19)], adv); w[2 * (i - 19) + 1] = vadd(w[2 * (i - 19) + 1], adv); }
      else { x[0] = vadd(x[0], adv); x[1] = vadd(x[1], adv); }
    }
    SB();
  }
}
struct G2 { u32x4 v[3], w[5]; u32x2 lo; };
struct A2 { LAS char* w[4]; LAS char* e; LAS char* v[2]; };
DI void shift(A2& a, int d) {
#pragma unroll
  for (int i = 0; i < 4; ++i) a.w[i] = vadd(a.w[i], d);
  a.e = vadd(a.e, d); a.v[0] = vadd(a.v[0], d); a.v[1] = vadd(a.v[1], d);
}
template <bool EXT> DI void rd2(int i, G2& f, const A2& a, int off, int u) {
  if (i < 3) f.v[i] = lds128(a.v[u] + off + 16384 + i * 2048);
  else if (i < 11) { const int ct = (i - 3) >> 1; if ((i - 3) & 1) f.w[ct] = cat(f.lo, trd(a.w[ct] + off + u * 8192 + 1024)); else f.lo = trd(a.w[ct] + off + u * 8192); }
  else if (EXT && i == 11) f.lo = trd(a.e + off + 28672 + u * 2048);
  else if (EXT && i == 12) f.w[4] = cat(f.lo, trd(a.e + off + 28672 + u * 2048 + 256));
}
template <bool E, bool FIRST, bool READ, bool PACK, int DBL>
DI void step2(f32x4 (&U)[3][5], const G2& f, G2& fn, const A2& a, int off, int u, u32x4 (&UA)[5], u32x2 (&UC)[5]) {
  const f32x4 z = {0.f, 0.f, 0.f, 0.f};
#pragma unroll
  for (int i = 0; i < (E ? 15 : 12); ++i) {
    const int ct = i / 3, kt = i % 3;
    if (PACK) { const u32x2 t = pk4(U[kt][ct]); if (kt == 0) { UA[ct][0] = t[0]; UA[ct][1] = t[1]; } else if (kt == 1) { UA[ct][2] = t[0]; UA[ct][3] = t[1]; } else UC[ct] = t; SB(); }
    U[kt][ct] = mfma(f.v[kt], f.w[ct], FIRST ? z : U[kt][ct]);
    if (READ) { if (i < DBL) { rd2<E>(2 * i, fn, a, off, u); rd2<E>(2 * i + 1, fn, a, off, u); } else rd2<E>(i + DBL, fn, a, off, u); }
    SB();
  }
}
template <bool PE, bool CE, bool READ, bool FIRST>
DI void finish2(f32x4 (&O)[5][3], f32x4 (&U)[3][5], const u32x4 (&A0)[5], const u32x2 (&C0)[5], G2& f0, G2& f1, const A2& a, int off, LAS char* p, LAS char* pc) {
  constexpr int N = PE ? 15 : 12;
  u32x4 pa0[3], pa1[3], pcc[3], A1[5]; u32x2 C1[5], lo[3];
#pragma unroll
  for (int i = 0; i < N; ++i) {
    U[i % 3][i / 3] = mfma(f1.v[i % 3], f1.w[i / 3], U[i % 3][i / 3]);
    if (i < 3) pa0[i] = lds128(p + i * 1024);
    else if (i < 6) pa1[i - 3] = lds128(p + 4608 + (i - 3) * 1024);
    else if (READ) rd2<CE>(i - 6, f0, a, off, 0);
    SB();
  }
#pragma unroll
  for (int i = 0; i < N; ++i) {
    const int ct = i / 3, n = i % 3;
    O[ct][n] = mfma(A0[ct], pa0[n], FIRST ? (f32x4){0.f, 0.f, 0.f, 0.f} : O[ct][n]);
    const u32x2 t = pk4(U[n][ct]);
    if (n == 0) { A1[ct][0] = t[0]; A1[ct][1] = t[1]; } else if (n == 1) { A1[ct][2] = t[0]; A1[ct][3] = t[1]; } else C1[ct] = t;
    SB();
  }
#pragma unroll
  for (int i = 0; i < N; ++i) {
    O[i / 3][i % 3] = mfma(A1[i / 3], pa1[i % 3], O[i / 3][i % 3]);
    if (i < 3) lo[i] = lds64(pc + i * 512);
    else if (i < 6) pcc[i - 3] = cat(lo[i - 3], lds64(pc + 4608 + (i - 3) * 512));
    else if (READ) rd2<CE>(i + N - 12, f0, a, off, 0);
    SB();
  }
  if (READ && N == 12) rd2<CE>(12, f0, a, off, 0);
#pragma unroll
  for (int i = 0; i < N; ++i) {
    O[i / 3][i % 3] = mfma(cat(C0[i / 3], C1[i / 3]), pcc[i % 3], O[i / 3][i % 3]);
    if (READ) rd2<CE>(i, f1, a, off, 1);
    SB();
  }
  if (READ && N == 12) rd2<CE>(12, f1, a, off, 1);
}
DI float max3(float a, float b, float c) { float r; asm("v_max3_f32 %0, %1, %2, %3" : "=v"(r) : "v"(a), "v"(b), "v"(c)); return r; }
DI float max2(float a, float b) { float r; asm("v_max_f32 %0, %1, %2" : "=v"(r) : "v"(a), "v"(b)); return r; }

DI void tail(Ctx& c, LAS char* sm, int wv, char* ws, char* out, f32x4 (&O)[5][3], bool xown) {
  const int lane = lane_id();
  const int hl = wv & 1, g = lane >> 4, m = lane & 15, b = 2 * c.bp + (wv >> 1);
  const u32 cu = blockIdx.x;
  const int sg = c.br * 8 + c.bp, me = c.rank * 4 + c.j;
  GAS u64* F3 = (GAS u64*)(ws + WS_F3) + sg * 256;
  const rsrc_t rm = mk(ws + WS_M, 1024u * 6144u);
  if (wv != c.rank) {
#pragma unroll
    for (int n = 0; n < 3; ++n)
#pragma unroll
      for (int c2 = 0; c2 < 2; ++c2) __builtin_amdgcn_raw_buffer_store_b128(cat(pk4(O[2 * c2][n]), pk4(O[2 * c2 + 1][n])), rm, lane * 16 + (n * 2 + c2) * 1024, (cu * 4 + wv) * 6144u, 16);
  }
  WAITV(0);
  STAMP(28);
  const u64 tok = *(LAS u64*)(sm + MISC + m * 8);
  {
    u32 old = 0;
    if (lane == 0) old = __hip_atomic_fetch_add((LAS u32*)(sm + MISC + 128), 1u, __ATOMIC_RELAXED, __HIP_MEMORY_SCOPE_WORKGROUP);
    old = __builtin_amdgcn_readfirstlane(old);
    if (old == 3u && lane == me) st_agent(F3 + me * 16, tok);
  }
  if (wv != c.rank) return;
  {
    const bool need = m != me && ((m & 3) == c.j || (xown && (m & 3) == (c.j ^ 2)));
    u32 spins = 0;
    while (!__all(!need || ld_agent(F3 + m * 16) == tok) && ++spins < SPIN_CAP) __builtin_amdgcn_s_sleep(1);
  }
  STAMP(29);
  u32x4 in[3][6];
#pragma unroll
  for (int r = 0; r < 3; ++r) {
    const u32 pr = (u32)((c.rank + 1 + r) & 3), pcu = (cu & ~6u) | (pr << 1);
#pragma unroll
    for (int i = 0; i < 6; ++i) in[r][i] = __builtin_amdgcn_raw_buffer_load_b128(rm, lane * 16 + i * 1024, (pcu * 4 + wv) * 6144u, 16);
  }
  const rsrc_t ro = mk(out, 768 * 2304);
  const u32 orow = (u32)(b * 48 + m) * 2304u + c.br * 1152;
  if (xown) {
    const rsrc_t rxx = mk(ws + WS_X, 1024u * 2048u);
    u32x2 xin[7][3];
#pragma unroll
    for (int r = 0; r < 7; ++r) {
      const u32 pr = (u32)((c.rank + 1 + r) & 3), pcu = ((cu & ~6u) | (pr << 1)) ^ (r >= 3 ? 16u : 0u);
#pragma unroll
      for (int n = 0; n < 3; ++n) xin[r][n] = __builtin_amdgcn_raw_buffer_load_b64(rxx, lane * 8 + n * 512, (pcu * 4 + wv) * 2048u, 16);
    }
#pragma unroll
    for (int n = 0; n < 3; ++n) {
      f32x4 a = O[4][n];
#pragma unroll
      for (int r = 0; r < 7; ++r) a += (f32x4){bflo(xin[r][n][0]), bfhi(xin[r][n][0]), bflo(xin[r][n][1]), bfhi(xin[r][n][1])};
      __builtin_amdgcn_raw_buffer_store_b64(pk4(a), ro, orow + n * (16 * 2304) + (512 + 32 * (c.j & 1) + 16 * hl + 4 * g) * 2, 0, 0);
    }
  }
#pragma unroll
  for (int n = 0; n < 3; ++n)
#pragma unroll
    for (int c2 = 0; c2 < 2; ++c2) {
      f32x4 a = O[2 * c2][n], d = O[2 * c2 + 1][n];
#pragma unroll
      for (int r = 0; r < 3; ++r) {
        const u32x4 v = in[r][n * 2 + c2];
        a += (f32x4){bflo(v[0]), bfhi(v[0]), bflo(v[1]), bfhi(v[1])};
        d += (f32x4){bflo(v[2]), bfhi(v[2]), bflo(v[3]), bfhi(v[3])};
      }
      const u32x2 pa = pk4(a), pd = pk4(d);
      auto s0 = __builtin_amdgcn_permlane16_swap(pa[0], pd[0], false, false);
      auto s1 = __builtin_amdgcn_permlane16_swap(pa[1], pd[1], false, false);
      __builtin_amdgcn_raw_buffer_store_b128((u32x4){s0[0], s1[0], s0[1], s1[1]}, ro,
          orow + n * (16 * 2304) + (128 * c.j + 64 * hl + 32 * c2 + 16 * (g & 1) + 8 * (g >> 1)) * 2, 0, 0);
    }
  STAMP(32);
}

DI void compute(Ctx& c, LAS char* sm, int wv) {
  start(c, sm, lane_id(), wv);
  char* ws = c.ws;
  const int bl = wv >> 1, hl = wv & 1;
  const u32 cu = blockIdx.x;
  int lane = lane_id(), g = lane >> 4, m = lane & 15, q = m >> 2, p = lane & 3;
  u32 wbase = (8 * g + q) * 256 + 32 * (q | ((g & 1) << 2)) + 8 * p;
  u32 xbase = (bl * 48 + m) * 128 + ((g ^ (m >> 1)) * 16);
  u32x4 Kf[3][4];
  f32x4 Q[8][3];
  u32x4 A1[8], B1[3];
  LAS char* aw[8]; LAS char* ax[2];
#pragma unroll
  for (int T = 0; T < 8; ++T) { A1[T] = (u32x4){0, 0, 0, 0}; PIN(A1[T]);
    aw[T] = vadd(sm + (wbase ^ (T << 5)), 12288 + hl * 16384);
#pragma unroll
    for (int n = 0; n < 3; ++n) { Q[T][n] = (f32x4){0.f, 0.f, 0.f, 0.f}; PIN(Q[T][n]); } }
#pragma unroll
  for (int n = 0; n < 3; ++n) { B1[n] = (u32x4){0, 0, 0, 0}; PIN(B1[n]); }
  ax[0] = vadd(sm + xbase, 0); ax[1] = vadd(sm + (xbase ^ 64), 0);
  SB();
  STAMP(33); WAITV(5); STAMP(34); BARRIER();
  STAMP(0);
#pragma unroll
  for (int kt = 0; kt < 3; ++kt) {
#pragma unroll 1
    for (int s = 0; s < 3; ++s) {
      if (s == 2) {
        const u32 l = lane_id(), kb = KS + wv * 4096 + (l & 15) * 256 + ((l >> 5) ^ (l & 15)) * 16 + ((l >> 1) & 8);
#pragma unroll
        for (int i = 0; i < 4; ++i) Kf[kt][i] = cat(lds64(sm + (kb ^ (i << 6))), lds64(sm + (kb ^ (i << 6) ^ 32)));
      }
      u32x4 A0[8], B0[3];
      half1<false>(Q, A1, B1, A0, B0, aw, ax, 0, 0);
      half1<true>(Q, A0, B0, A1, B1, aw, ax, 1, s == 2 ? -2 * QP : QP);
      if (kt == 0 && s == 0) WAITV(0);
      LBARRIER();
      STAMP(1 + 3 * kt + s);
    }
  }
  SB();
  lane = lane_id(); g = lane >> 4; m = lane & 15; q = m >> 2; p = lane & 3;
  wbase = (8 * g + q) * 256 + 32 * (q | ((g & 1) << 2)) + 8 * p;
  xbase = (bl * 48 + m) * 128 + ((g ^ (m >> 1)) * 16);
  A2 a;
#pragma unroll
  for (int ct = 0; ct < 4; ++ct) a.w[ct] = sm + (wbase ^ ((4 * hl + ct) << 5));
  a.e = sm + (8 * g + q) * 64 + 32 * (hl ^ (g & 1)) + 8 * p;
  a.v[0] = sm + xbase; a.v[1] = sm + (xbase ^ 64);
  G2 f0, f1;
  f32x4 O[5][3], U[3][5];
  {
    u32x4 qf[3][4];
    f32x4 S[3][3];
#pragma unroll
    for (int i = 0; i < 24; ++i) {
      Q[i & 7][i >> 3] = mfma(A1[i & 7], B1[i >> 3], Q[i & 7][i >> 3]);
      if (i >= 8) { const u32x2 t = pk4(Q[i & 7][(i >> 3) - 1]); qf[(i >> 3) - 1][(i & 7) >> 1][2 * (i & 1)] = t[0]; qf[(i >> 3) - 1][(i & 7) >> 1][2 * (i & 1) + 1] = t[1]; }
      SB();
    }
    STAMP(36);
#pragma unroll
    for (int i = 0; i < 36; ++i) {
      const int n = i / 12, s = (i % 12) / 3, kt = i % 3;
      S[n][kt] = mfma(Kf[kt][s], qf[n][s], s == 0 ? (f32x4){0.f, 0.f, 0.f, 0.f} : S[n][kt]);
      if (i >= 2 && i < 10) { const u32x2 t = pk4(Q[i - 2][2]); qf[2][(i - 2) >> 1][2 * (i & 1)] = t[0]; qf[2][(i - 2) >> 1][2 * (i & 1) + 1] = t[1]; }
      if (i >= 10 && i < 23) rd2<true>(i - 10, f0, a, 0, 0);
      if (i >= 23) rd2<true>(i - 23, f1, a, 0, 1);
      SB();
    }
    STAMP(37);
    const rsrc_t rp = mk(ws + WS_P, 256u * 18432u);
    const u32 pso = cu * 18432u + wv * 4608;
    LAS char* pl = vadd(sm + lane * 16, P_A + wv * 4608);
    LAS char* pl2 = vadd(sm + lane * 8, P_A + wv * 4608 + 3072);
    float mx[3], sum[3];
    int um = 0;
#define UM() do { if (um < 15) { U[um % 3][um / 3] = mfma(f0.v[um % 3], f0.w[um / 3], (f32x4){0.f, 0.f, 0.f, 0.f}); ++um; } SB(); } while (0)
#pragma unroll
    for (int n = 0; n < 3; ++n)
      mx[n] = max3(max3(S[n][0][0], S[n][0][1], S[n][0][2]), max3(S[n][0][3], S[n][1][0], S[n][1][1]),
                   max3(max3(S[n][1][2], S[n][1][3], S[n][2][0]), max3(S[n][2][1], S[n][2][2], S[n][2][3]), S[n][0][0]));
#pragma unroll
    for (int n = 0; n < 3; ++n) { auto r = __builtin_amdgcn_permlane16_swap(__float_as_uint(mx[n]), __float_as_uint(mx[n]), false, false); mx[n] = max2(__uint_as_float(r[0]), __uint_as_float(r[1])); }
#pragma unroll
    for (int n = 0; n < 3; ++n) { auto r = __builtin_amdgcn_permlane32_swap(__float_as_uint(mx[n]), __float_as_uint(mx[n]), false, false); mx[n] = max2(__uint_as_float(r[0]), __uint_as_float(r[1])) * -0.12751743344863963f; }
    SB();
#pragma unroll
    for (int n = 0; n < 3; ++n)
#pragma unroll
      for (int kt = 0; kt < 3; ++kt) {
#pragma unroll
        for (int i = 0; i < 4; ++i) S[n][kt][i] = __builtin_amdgcn_exp2f(__builtin_fmaf(S[n][kt][i], 0.12751743344863963f, mx[n]));
        UM();
      }
    STAMP(38);
#pragma unroll
    for (int n = 0; n < 3; ++n) { const f32x4 s4 = S[n][0] + S[n][1] + S[n][2]; sum[n] = (s4[0] + s4[1]) + (s4[2] + s4[3]); UM(); }
#pragma unroll
    for (int n = 0; n < 3; ++n) { auto r = __builtin_amdgcn_permlane16_swap(__float_as_uint(sum[n]), __float_as_uint(sum[n]), false, false); sum[n] = __uint_as_float(r[0]) + __uint_as_float(r[1]); }
#pragma unroll
    for (int n = 0; n < 3; ++n) { auto r = __builtin_amdgcn_permlane32_swap(__float_as_uint(sum[n]), __float_as_uint(sum[n]), false, false); sum[n] = __builtin_amdgcn_rcpf(__uint_as_float(r[0]) + __uint_as_float(r[1])); }
    SB();
    STAMP(39);
#pragma unroll
    for (int n = 0; n < 3; ++n) {
      const u32x4 pa = cat(pk4(S[n][0] * sum[n]), pk4(S[n][1] * sum[n]));
      const u32x2 pc = pk4(S[n][2] * sum[n]);
      __builtin_amdgcn_raw_buffer_store_b128(pa, rp, lane * 16 + n * 1024, pso, 16);
      __builtin_amdgcn_raw_buffer_store_b64(pc, rp, lane * 8 + 3072 + n * 512, pso, 16);
      *(LAS u32x4*)(pl + n * 1024) = pa;
      *(LAS u32x2*)(pl2 + n * 512) = pc;
      UM();
    }
  }
  STAMP(26);
  SB();
  LAS char* pp = sm + bl * 9216 + lane * 16;
  LAS char* pq = sm + bl * 9216 + lane * 8 + 3072;
  u32x4 UA[5]; u32x2 UC[5];
  const bool xown = (wv == c.rank) && ((c.j >> 1) == (c.rank & 1));
#define ENDI(gi) do { if ((gi) % 3 == 0) shift(a, RP); if ((gi) % 3 == 2) shift(a, -RP); if ((gi) + 1 == XB) WAITV(0); LBARRIER(); STAMP(1 + (gi)); SB(); } while (0)
#define SLOT(gi) ((gi) % 3 == 2 ? RP : 0)
#define BODY(t, PE, CE) do { \
    constexpr int g0 = 9 + 4 * (t); \
    if ((t) > 0) { \
      constexpr int pa_ = (t) == 1 ? P_A : (t) == 2 ? P_B : P_C; \
      finish2<PE, CE, true, (t) == 1>(O, U, UA, UC, f0, f1, a, SLOT(g0), vadd(pp, pa_), vadd(pq, pa_)); \
      if ((t) == 2 && !xown) { const rsrc_t rxx = mk(ws + WS_X, 1024u * 2048u); \
        _Pragma("unroll") for (int n = 0; n < 3; ++n) __builtin_amdgcn_raw_buffer_store_b64(pk4(O[4][n]), rxx, lane * 8 + n * 512, (cu * 4 + wv) * 2048u, 16); } \
      step2<CE, true, false, false, 0>(U, f0, f1, a, 0, 0, UA, UC); } \
    ENDI(g0); \
    step2<CE, false, true, false, 0>(U, f1, f0, a, SLOT(g0 + 1), 0, UA, UC); \
    step2<CE, false, true, false, 3>(U, f0, f1, a, SLOT(g0 + 1), 1, UA, UC); ENDI(g0 + 1); \
    step2<CE, false, true, false, 0>(U, f1, f0, a, SLOT(g0 + 2), 0, UA, UC); \
    step2<CE, true, true, true, 3>(U, f0, f1, a, SLOT(g0 + 2), 1, UA, UC); ENDI(g0 + 2); \
    step2<CE, false, true, false, 0>(U, f1, f0, a, SLOT(g0 + 3), 0, UA, UC); \
    step2<CE, false, true, false, 3>(U, f0, f1, a, SLOT(g0 + 3), 1, UA, UC); ENDI(g0 + 3); \
  } while (0)
  BODY(0, true, true);
  BODY(1, true, true);
  BODY(2, true, false);
  BODY(3, false, false);
  finish2<false, false, false, false>(O, U, UA, UC, f0, f1, a, 0, vadd(pp, P_A), vadd(pq, P_A));
  STAMP(27);
  tail(c, sm, wv, ws, c.out, O, xown);
}

__global__ __launch_bounds__(512, 2) void fused_attn_block(const char* xA, const char* kA, const char* vA, const char* wqA, const char* woA,
                                                          const char* xB, const char* kB, const char* vB, const char* wqB, const char* woB,
                                                          char* out, char* ws) {
  __shared__ __attribute__((aligned(256))) char smem[LDS_BYTES];
  LAS char* sm = (LAS char*)smem;
  const int wv = __builtin_amdgcn_readfirstlane(threadIdx.x >> 6);
  Ctx c;
  c.stamps = 0; c.ta = 0; c.tb = 0;
  STAMP(63);
  const int id = blockIdx.x;
  c.br = id & 1; c.rank = (id >> 1) & 3; c.j = (id >> 3) & 3; c.bp = id >> 5;
  c.h0 = c.rank * 8; c.kvsh = 1 - c.br;
  {
    const u64 ka = (u64)(uintptr_t)__builtin_amdgcn_kernarg_segment_ptr();
    asm volatile("s_load_dwordx2 %0, %8, 0x0\n\ts_load_dwordx2 %1, %8, 0x18\n\ts_load_dwordx2 %2, %8, 0x8\n\ts_load_dwordx2 %3, %8, 0x10\n\t"
                 "s_load_dwordx2 %4, %8, 0x20\n\ts_load_dwordx2 %5, %9, 0x50\n\ts_load_dwordx2 %6, %9, 0x58\n\ts_getpc_b64 %7"
                 : "=&s"(c.a[0]), "=&s"(c.a[1]), "=&s"(c.a[2]), "=&s"(c.a[3]), "=&s"(c.a[4]), "=&s"(c.a[5]), "=&s"(c.a[6]), "=&s"(c.pc)
                 : "s"(ka + c.br * 40), "s"(ka) : "memory");
  }
  if (wv == 4) loader<0>(c, sm);
  else if (wv == 5) loader<1>(c, sm);
  else if (wv == 6) loader<2>(c, sm);
  else if (wv == 7) comm(c, sm);
  else if (ABL_NOCOMP) { start(c, sm, lane_id(), wv); STAMP(33); WAITV(5); STAMP(34); BARRIER(); STAMP(0); WAITV(0); for (int g = 1; g < 26; ++g) { BARRIER(); STAMP(g); } }
  else compute(c, sm, wv);
  STAMP(62);
  if (wv == c.rank) { const u64 t0 = __builtin_amdgcn_s_memrealtime(); const u64 d = (u64)(c.tb - c.ta) / 2u; while (__builtin_amdgcn_s_memrealtime() - t0 < d) __builtin_amdgcn_s_sleep(4); }
}

extern "C" void kernel_launch(void* const* d_in, const int* in_sizes, int n_in, void* d_out, int out_size, void* d_ws, size_t ws_size,
                              hipStream_t stream) {
  const char* const* in = (const char* const*)d_in;
  fused_attn_block<<<256, 512, 0, stream>>>(in[0], in[1], in[2], in[3], in[4], in[5], in[6], in[7], in[8], in[9], (char*)d_out, (char*)d_ws);
}
```

```cpp
#include <hip/hip_runtime.h>
#include <stdint.h>

typedef unsigned u32;
typedef unsigned long long u64;
typedef u32 u32x4 __attribute__((ext_vector_type(4)));
typedef u32 u32x2 __attribute__((ext_vector_type(2)));
typedef u32 u32x8 __attribute__((ext_vector_type(8)));
typedef float f32x4 __attribute__((ext_vector_type(4)));
typedef float f32x2 __attribute__((ext_vector_type(2)));
typedef short s16x4 __attribute__((ext_vector_type(4)));
typedef __bf16 bf16x8 __attribute__((ext_vector_type(8)));
typedef __bf16 bf16x2 __attribute__((ext_vector_type(2)));
#define LAS __attribute__((address_space(3)))
#define GAS __attribute__((address_space(1)))
#define DI __device__ __forceinline__
typedef __amdgpu_buffer_rsrc_t rsrc_t;

#define DIAG 0
#define CODEPF 35
#define XB 11
#define ABL_NOLOAD 0
#define ABL_NOCOMP 0

constexpr int QP = 45056;
constexpr int RP = 32768;
constexpr int KS = 135168;
constexpr int P_A = 98304, P_B = 116736, P_C = 135168;
constexpr int MISC = 153600;
constexpr int JUNK = 153856;
constexpr int LDS_BYTES = 154112;
constexpr u32 WS_F1 = 0;
constexpr u32 WS_F3 = 32768;
constexpr u32 WS_R = 65536;
constexpr u32 WS_P = 131072;
constexpr u32 WS_M = WS_P + 256u * 18432u;
constexpr u32 WS_X = WS_M + 1024u * 6144u;
constexpr u32 WS_DIAG = 16u << 20;
constexpr u64 TOKK = 0x9E3779B97F4A7C15ull;
constexpr u32 SPIN_CAP = 1u << 20;

DI rsrc_t mk(const void* p, u32 bytes) { return __builtin_amdgcn_make_buffer_rsrc((void*)p, 0, bytes, 0x00020000); }
DI void dma(rsrc_t r, LAS char* dst, u32 voff, u32 soff) { if (ABL_NOLOAD) return; __builtin_amdgcn_raw_ptr_buffer_load_lds(r, (LAS void*)dst, 16, voff, soff, 0, 0); }
DI void dma_asm(rsrc_t r, u32 dst, u32 voff, u32 soff) {
  if (ABL_NOLOAD) return;
  asm volatile("s_mov_b32 m0, %0\n\ts_nop 0\n\tbuffer_load_dwordx4 %1, %2, %3 offen lds" :: "s"(dst), "v"(voff), "s"(r), "s"(soff) : "memory", "m0");
}
#define WAITV(n) asm volatile("s_waitcnt vmcnt(%0)" ::"i"(n) : "memory")
#define BARRIER() asm volatile("s_barrier" ::: "memory")
#define LBARRIER() asm volatile("s_waitcnt lgkmcnt(0)\n\ts_barrier" ::: "memory")
#define SB() __builtin_amdgcn_sched_barrier(0)
#define PIN(x) asm volatile("" : "+v"(x))
DI int lane_id() { int l = __builtin_amdgcn_mbcnt_hi(~0u, __builtin_amdgcn_mbcnt_lo(~0u, 0u)); PIN(l); return l; }
DI u32 vmcnt_now() { u32 v; asm volatile("s_getreg_b32 %0, hwreg(HW_REG_IB_STS)" : "=s"(v) :: "memory"); return v & 0x00C0000Fu; }
DI f32x4 mfma(u32x4 a, u32x4 b, f32x4 c) { return __builtin_amdgcn_mfma_f32_16x16x32_bf16(__builtin_bit_cast(bf16x8, a), __builtin_bit_cast(bf16x8, b), c, 0, 0, 0); }
DI u32 pk(float a, float b) { f32x2 f = {a, b}; return __builtin_bit_cast(u32, __builtin_convertvector(f, bf16x2)); }
DI u32x2 pk4(f32x4 v) { u32x2 r = {pk(v[0], v[1]), pk(v[2], v[3])}; return r; }
DI u32x4 cat(u32x2 a, u32x2 b) { u32x4 r = {a[0], a[1], b[0], b[1]}; return r; }
DI u32x2 trd(LAS char* p) { return __builtin_bit_cast(u32x2, __builtin_amdgcn_ds_read_tr16_b64_v4i16((LAS s16x4*)p)); }
DI u32x4 lds128(LAS char* p) { return *(LAS u32x4*)p; }
DI u32x2 lds64(LAS char* p) { return *(LAS u32x2*)p; }
DI float bflo(u32 x) { return __builtin_bit_cast(float, x << 16); }
DI float bfhi(u32 x) { return __builtin_bit_cast(float, x & 0xffff0000u); }
DI u64 ld_agent(GAS u64* p) { return __hip_atomic_load(p, __ATOMIC_RELAXED, __HIP_MEMORY_SCOPE_AGENT); }
DI void st_agent(GAS u64* p, u64 v) { __hip_atomic_store(p, v, __ATOMIC_RELAXED, __HIP_MEMORY_SCOPE_AGENT); }
DI u64 rdlane64(u64 v, int l) { return ((u64)(u32)__builtin_amdgcn_readlane((int)(v >> 32), l) << 32) | (u32)__builtin_amdgcn_readlane((int)v, l); }
DI LAS char* vadd(LAS char* p, int d) { u32 r; asm volatile("v_add_u32 %0, %1, %2" : "=v"(r) : "s"(d), "v"((u32)(uintptr_t)p)); return (LAS char*)(uintptr_t)r; }
DI int wlane(int val, int idx, int old) { asm volatile("s_mov_b32 m0, %2\n\ts_nop 0\n\tv_writelane_b32 %0, %1, m0" : "+v"(old) : "s"(val), "s"(idx) : "m0"); return old; }
#define STAMP(i) do {} while (0)

struct Ctx {
  const char *x, *k, *v, *wq, *wo;
  u64 a[7], pc;
  char *out, *ws;
  int stamps; u32 ta, tb;
  int br, rank, bp, j, h0, kvsh;
};

DI void start(Ctx& c, LAS char* sm, int lane, int wv) {
  asm volatile("s_waitcnt lgkmcnt(0)\n\ts_mov_b64 %0, %7\n\ts_mov_b64 %1, %8\n\ts_mov_b64 %2, %9\n\ts_mov_b64 %3, %10\n\ts_mov_b64 %4, %11\n\ts_mov_b64 %5, %12\n\ts_mov_b64 %6, %13"
               : "=&s"(c.x), "=&s"(c.wq), "=&s"(c.k), "=&s"(c.v), "=&s"(c.wo), "=&s"(c.out), "=&s"(c.ws)
               : "s"(c.a[0]), "s"(c.a[1]), "s"(c.a[2]), "s"(c.a[3]), "s"(c.a[4]), "s"(c.a[5]), "s"(c.a[6]) : "memory");
  const rsrc_t rx = mk(c.x, 768 * 1152), rq = mk(c.wq, 576 * 8192);
  const u32 l4 = lane >> 4;
  const u32 vx = (lane >> 3) * 1152 + ((lane & 7) ^ (l4 | ((wv & 1) << 2))) * 16;
  const u32 vq = l4 * 8192 + ((lane & 15) ^ ((l4 | ((wv & 2) << 1)) << 1)) * 16;
  const u32 xs = (u32)(c.bp * 96 + wv * 8) * 1152u, qs = (u32)(c.h0 + 2 * c.j) * 256u + wv * (4 * 8192);
  const u32 d = (u32)(uintptr_t)sm + wv * 1024;
  dma_asm(rx, d, vx, xs);
  dma_asm(rx, (u32)(uintptr_t)sm + (wv >> 2) * QP + (8 + (wv & 3)) * 1024, vx, (u32)(c.bp * 96 + 64 + (wv & 3) * 8) * 1152u + (wv >> 2) * 128);
#pragma unroll
  for (int i = 0; i < 4; ++i) dma_asm(rq, d + 12288 + i * 8192, vq, qs + (i & 1) * (32 * 8192) + (i >> 1) * 256);
  dma_asm(rx, d + QP, vx, xs + 128);
#pragma unroll
  for (int i = 0; i < 4; ++i) dma_asm(rq, d + QP + 12288 + i * 8192, vq, qs + 64 * 8192 + (i & 1) * (32 * 8192) + (i >> 1) * 256);
  STAMP(30);
}

template <int L> struct Loader {
  rsrc_t rx, rq, ro, rv, rk;
  u32 vx[2], vq[2], vo[2], vv[2], ve, vk;
  u32 xso, qso, oso, vso0, vso1, eso, kso[4];
  int hown, h0, kvsh;
  static constexpr int N1 = 4 + (L == 2 ? 10 : 11);
  static constexpr int NK = L == 2 ? 6 : 5;
  static constexpr int N2N = 4 + (L == 0 ? 6 : 5);
  static constexpr int N2E = N2N + (L == 0 ? 0 : 2);
  DI void p1(int ks, LAS char* q) {
    const u32 xs = xso + ks * 128, ws_ = qso + ks * (64 * 8192);
#pragma unroll
    for (int i = 0; i < 4; ++i) { const int p = L + 3 * i; dma(rx, q + p * 1024, vx[p & 1], xs + p * (8 * 1152)); }
#pragma unroll
    for (int i = 0; i < 11; ++i) { const int p = L + 3 * i; if (p < 32) dma(rq, q + 12288 + p * 1024, vq[(p >> 1) & 1], ws_ + (p & 15) * (4 * 8192) + (p >> 4) * 256); }
  }
  DI void pk_(int kt, LAS char* d) {
#pragma unroll
    for (int i = 0; i < 6; ++i) { const int p = (2 - L) + 3 * i; if (p < 16) dma(rk, d + p * 1024, vk ^ ((p & 3) << 6), kso[p >> 2] + kt * 4096 + (p & 3) * 1024); }
  }
  template <bool EXT> DI void p2(int st, LAS char* r) {
    const int h = h0 + ((hown + (st >> 1)) & 7);
    const u32 row = (u32)(h * 128 + (st & 1) * 64) * 1152u;
    const u32 vh = (u32)(h >> kvsh) * (48 * 256) + (st & 1) * 128;
#pragma unroll
    for (int i = 0; i < 4; ++i) { const int p = L + 3 * i; dma(rv, r + 16384 + p * 1024, vv[p & 1], (p < 6 ? vso0 : vso1) + vh + (p % 6) * 2048); }
#pragma unroll
    for (int i = 0; i < 6; ++i) { const int p = L + 3 * i; if (p < 16) dma(ro, r + p * 1024, vo[(p >> 1) & 1], oso + row + p * (4 * 1152)); }
    if (EXT && L > 0) {
#pragma unroll
      for (int i = 0; i < 2; ++i) { const int p = (L - 1) + 2 * i; dma(ro, r + 28672 + p * 1024, ve, eso + row + p * (16 * 1152)); }
    }
  }
};

template <int L> DI void loader(Ctx& c, LAS char* sm) {
  const int lane = lane_id();
  start(c, sm, lane, 4 + L);
  Loader<L> z;
  const int hkv = 32 >> c.kvsh;
  z.rx = mk(c.x, 768 * 1152); z.rq = mk(c.wq, 576 * 8192); z.ro = mk(c.wo, 4096 * 1152);
  z.rv = mk(c.v, (u32)(16 * hkv) * (48 * 256)); z.rk = mk(c.k, (u32)(16 * hkv) * (48 * 256));
  const u32 l3 = lane >> 3, c8 = lane & 7, l4 = lane >> 4, c16 = lane & 15;
  const u32 cx0 = (c8 ^ l4) * 16, cx1 = (c8 ^ (l4 | 4)) * 16, cw0 = (c16 ^ (l4 << 1)) * 16, cw1 = (c16 ^ ((l4 | 4) << 1)) * 16;
  z.vx[0] = l3 * 1152 + cx0; z.vx[1] = l3 * 1152 + cx1;
  z.vq[0] = l4 * 8192 + cw0; z.vq[1] = l4 * 8192 + cw1;
  z.xso = (u32)(c.bp * 96) * 1152u;
  z.hown = 2 * c.j; z.h0 = c.h0; z.kvsh = c.kvsh;
  z.qso = (u32)(c.h0 + z.hown) * 256u;
  z.vo[0] = l4 * 1152 + cw0; z.vo[1] = l4 * 1152 + cw1;
  z.vv[0] = l3 * 256 + cx0; z.vv[1] = l3 * 256 + cx1;
  z.vk = l4 * 256 + (c16 ^ l4) * 16;
  z.ve = (lane >> 2) * 1152 + ((lane & 3) ^ ((lane >> 5) << 1)) * 16;
  z.oso = c.j * 256; z.eso = 1024 + (c.j & 1) * 64;
  z.vso0 = (u32)(2 * c.bp * hkv) * (48 * 256); z.vso1 = z.vso0 + (u32)hkv * (48 * 256);
#pragma unroll
  for (int w = 0; w < 4; ++w) z.kso[w] = ((w >> 1) ? z.vso1 : z.vso0) + (u32)((c.h0 + z.hown + (w & 1)) >> c.kvsh) * (48 * 256);
  STAMP(33); WAITV(5); STAMP(34); BARRIER();
  int slot = 2;
#pragma unroll 1
  for (int s = 2; s < 9; ++s) {
    z.p1(s, sm + slot * QP);
    if (slot == 2) { z.pk_(s / 3, sm + KS); WAITV(Loader<L>::N1 + Loader<L>::NK); slot = 0; } else { WAITV(Loader<L>::N1); ++slot; }
    BARRIER();
  }
#pragma unroll 1
  for (int s = 0; s < 8; ++s) { z.template p2<true>(s, sm + slot * RP); slot = slot == 2 ? 0 : slot + 1; WAITV(Loader<L>::N2E); BARRIER(); }
#pragma unroll 1
  for (int s = 8; s < 16; ++s) { z.template p2<false>(s, sm + slot * RP); slot = slot == 2 ? 0 : slot + 1; WAITV(Loader<L>::N2N); BARRIER(); }
  WAITV(0); BARRIER();
  STAMP(31);
}

struct Pace {
  int g, budget;
  DI void bar() { BARRIER(); ++g; budget = 4; }
  DI void idle(int dead) {
    for (;;) {
      if (vmcnt_now() == 0u) break;
      if (budget > 0) { __builtin_amdgcn_s_sleep(1); --budget; }
      else if (g < dead) bar();
      else break;
    }
    WAITV(0);
  }
};
DI u64 ld_async(const char* base, u32 off) { u64 r; asm volatile("global_load_dwordx2 %0, %1, %2 sc1" : "=v"(r) : "v"(off), "s"(base) : "memory"); return r; }

DI void comm(Ctx& c, LAS char* sm) {
  const int lane = lane_id();
  start(c, sm, lane, 7);
  char* ws = c.ws;
  if (CODEPF) {
    u32 line = ((blockIdx.x >> 3) % 5) * 64 + lane; line = line < CODEPF * 8 ? line : 0u;
    asm volatile("s_mov_b32 m0, %0\n\ts_nop 0\n\tglobal_load_lds_dword %1, %2" :: "s"((u32)(uintptr_t)sm + JUNK), "v"(line * 128), "s"(c.pc & ~255ull) : "memory", "m0");
  }
  const int sg = c.br * 8 + c.bp, me = c.rank * 4 + c.j, m = lane & 15;
  const char* F1 = ws + WS_F1 + sg * 2048;
  const char* R = ws + WS_R + sg * 2048;
  Pace pc;
  __builtin_amdgcn_s_setprio(3);
  STAMP(33); WAITV(5 + (CODEPF ? 1 : 0)); STAMP(34); BARRIER();
  u64 tok = ld_async(F1, m * 128);
  WAITV(1);
  pc.g = 1; pc.bar();
  pc.idle(6); PIN(tok);
  tok += TOKK;
  if (lane < 16) { st_agent((GAS u64*)(R + m * 128 + me * 8), tok); *(LAS u64*)(sm + MISC + m * 8) = tok; }
  if (lane == 0) *(LAS u32*)(sm + MISC + 128) = 0u;
  const u64 mytok = rdlane64(tok, me);
  asm volatile("s_waitcnt lgkmcnt(0)" ::: "memory");
  while (pc.g < 8) pc.bar();
  for (u32 spins = 0; spins < SPIN_CAP; ++spins) {
    u64 r = ld_async(R, me * 128 + m * 8);
    pc.idle(XB + 1); PIN(r);
    if (__all(r == mytok)) break;
  }
  while (pc.g <= XB) pc.bar();
  if (lane == 0) st_agent((GAS u64*)(F1 + me * 128), mytok);
  STAMP(40);
  const rsrc_t rp = mk(ws + WS_P, 256u * 18432u);
  const u32 foff = (u32)(c.rank * 4 + (lane & 3)) * 128;
  const u64 want = __shfl(tok, c.rank * 4 + (lane & 3));
  u32 seen = 0;
#pragma unroll 1
  for (int t = 1; t < 4; ++t) {
    const int pj = (c.j + t) & 3, dead = 13 + 4 * t;
    for (u32 spins = 0; !((seen >> pj) & 1u) && spins < SPIN_CAP; ++spins) {
      u64 r = ld_async(F1, foff);
      do { if (pc.g < dead - 1) pc.bar(); } while (vmcnt_now() != 0u);
      WAITV(0); PIN(r);
      seen = (u32)__ballot(r == want) & 15u;
    }
    STAMP(40 + 2 * t - 1);
    const u32 so = (u32)((c.bp * 4 + pj) * 8 + c.rank * 2 + c.br) * 18432u;
    u32x4 pv[18];
#pragma unroll
    for (int i = 0; i < 18; ++i) asm volatile("buffer_load_dwordx4 %0, %1, %2, %3 offen offset:%4 sc1" : "=v"(pv[i]) : "v"(lane * 16 + (i >> 2) * 4096), "s"(rp), "s"(so), "i"((i & 3) * 1024) : "memory");
    STAMP(47 + 3 * t);
    do { if (pc.g < dead) pc.bar(); } while (vmcnt_now() != 0u || (t == 3 && pc.g <= 14));
    WAITV(0);
    STAMP(48 + 3 * t);
    LAS char* dst = sm + (t == 1 ? P_B : t == 2 ? P_C : P_A) + lane * 16;
#pragma unroll
    for (int i = 0; i < 18; ++i) { PIN(pv[i]); *(LAS u32x4*)(dst + i * 1024) = pv[i]; }
    asm volatile("s_waitcnt lgkmcnt(0)" ::: "memory");
    STAMP(40 + 2 * t);
    if (pc.g <= dead) pc.bar();
  }
  while (pc.g <= 25) pc.bar();
}

template <bool ADV> DI void half1(f32x4 (&Q)[8][3], const u32x4 (&A)[8], const u32x4 (&B)[3], u32x4 (&An)[8], u32x4 (&Bn)[3],
                                  LAS char* (&w)[8], LAS char* (&x)[2], int u, int adv) {
  u32x2 lo[8];
#pragma unroll
  for (int i = 0; i < 24; ++i) {
    Q[i / 3][i % 3] = mfma(A[i / 3], B[i % 3], Q[i / 3][i % 3]);
    constexpr int what[19] = {70, 71, 00, 01, 100, 10, 11, 101, 20, 21, 102, 30, 31, 60, 40, 41, 61, 50, 51};
    if (i < 19) {
      const int k = what[i];
      if (k >= 100) Bn[k - 100] = lds128(x[u] + (k - 100) * 2048);
      else if (k % 10 == 0) lo[k / 10] = trd(w[k / 10] + u * 8192);
      else An[k / 10] = cat(lo[k / 10], trd(w[k / 10] + u * 8192 + 1024));
    } else if (ADV) {
      if (i < 23) { w[2 * (i - 19)] = vadd(w[2 * (i - 19)], adv); w[2 * (i - 19) + 1] = vadd(w[2 * (i - 19) + 1], adv); }
      else { x[0] = vadd(x[0], adv); x[1] = vadd(x[1], adv); }
    }
    SB();
  }
}
struct G2 { u32x4 v[3], w[5]; u32x2 lo; };
struct A2 { LAS char* w[4]; LAS char* e; LAS char* v[2]; };
DI void shift(A2& a, int d) {
#pragma unroll
  for (int i = 0; i < 4; ++i) a.w[i] = vadd(a.w[i], d);
  a.e = vadd(a.e, d); a.v[0] = vadd(a.v[0], d); a.v[1] = vadd(a.v[1], d);
}
template <bool EXT> DI void rd2(int i, G2& f, const A2& a, int off, int u) {
  if (i < 3) f.v[i] = lds128(a.v[u] + off + 16384 + i * 2048);
  else if (i < 11) { const int ct = (i - 3) >> 1; if ((i - 3) & 1) f.w[ct] = cat(f.lo, trd(a.w[ct] + off + u * 8192 + 1024)); else f.lo = trd(a.w[ct] + off + u * 8192); }
  else if (EXT && i == 11) f.lo = trd(a.e + off + 28672 + u * 2048);
  else if (EXT && i == 12) f.w[4] = cat(f.lo, trd(a.e + off + 28672 + u * 2048 + 256));
}
template <bool E, bool FIRST, bool READ, bool PACK, int DBL>
DI void step2(f32x4 (&U)[3][5], const G2& f, G2& fn, const A2& a, int off, int u, u32x4 (&UA)[5], u32x2 (&UC)[5]) {
  const f32x4 z = {0.f, 0.f, 0.f, 0.f};
#pragma unroll
  for (int i = 0; i < (E ? 15 : 12); ++i) {
    const int ct = i / 3, kt = i % 3;
    if (PACK) { const u32x2 t = pk4(U[kt][ct]); if (kt == 0) { UA[ct][0] = t[0]; UA[ct][1] = t[1]; } else if (kt == 1) { UA[ct][2] = t[0]; UA[ct][3] = t[1]; } else UC[ct] = t; SB(); }
    U[kt][ct] = mfma(f.v[kt], f.w[ct], FIRST ? z : U[kt][ct]);
    if (READ) { if (i < DBL) { rd2<E>(2 * i, fn, a, off, u); rd2<E>(2 * i + 1, fn, a, off, u); } else rd2<E>(i + DBL, fn, a, off, u); }
    SB();
  }
}
template <bool PE, bool CE, bool READ, bool FIRST>
DI void finish2(f32x4 (&O)[5][3], f32x4 (&U)[3][5], const u32x4 (&A0)[5], const u32x2 (&C0)[5], G2& f0, G2& f1, const A2& a, int off, LAS char* p, LAS char* pc) {
  constexpr int N = PE ? 15 : 12;
  u32x4 pa0[3], pa1[3], pcc[3], A1[5]; u32x2 C1[5], lo[3];
#pragma unroll
  for (int i = 0; i < N; ++i) {
    U[i % 3][i / 3] = mfma(f1.v[i % 3], f1.w[i / 3], U[i % 3][i / 3]);
    if (i < 3) pa0[i] = lds128(p + i * 1024);
    else if (i < 6) pa1[i - 3] = lds128(p + 4608 + (i - 3) * 1024);
    else if (READ) rd2<CE>(i - 6, f0, a, off, 0);
    SB();
  }
#pragma unroll
  for (int i = 0; i < N; ++i) {
    const int ct = i / 3, n = i % 3;
    O[ct][n] = mfma(A0[ct], pa0[n], FIRST ? (f32x4){0.f, 0.f, 0.f, 0.f} : O[ct][n]);
    const u32x2 t = pk4(U[n][ct]);
    if (n == 0) { A1[ct][0] = t[0]; A1[ct][1] = t[1]; } else if (n == 1) { A1[ct][2] = t[0]; A1[ct][3] = t[1]; } else C1[ct] = t;
    SB();
  }
#pragma unroll
  for (int i = 0; i < N; ++i) {
    O[i / 3][i % 3] = mfma(A1[i / 3], pa1[i % 3], O[i / 3][i % 3]);
    if (i < 3) lo[i] = lds64(pc + i * 512);
    else if (i < 6) pcc[i - 3] = cat(lo[i - 3], lds64(pc + 4608 + (i - 3) * 512));
    else if (READ) rd2<CE>(i + N - 12, f0, a, off, 0);
    SB();
  }
  if (READ && N == 12) rd2<CE>(12, f0, a, off, 0);
#pragma unroll
  for (int i = 0; i < N; ++i) {
    O[i / 3][i % 3] = mfma(cat(C0[i / 3], C1[i / 3]), pcc[i % 3], O[i / 3][i % 3]);
    if (READ) rd2<CE>(i, f1, a, off, 1);
    SB();
  }
  if (READ && N == 12) rd2<CE>(12, f1, a, off, 1);
}
DI float max3(float a, float b, float c) { float r; asm("v_max3_f32 %0, %1, %2, %3" : "=v"(r) : "v"(a), "v"(b), "v"(c)); return r; }
DI float max2(float a, float b) { float r; asm("v_max_f32 %0, %1, %2" : "=v"(r) : "v"(a), "v"(b)); return r; }

DI void tail(Ctx& c, LAS char* sm, int wv, char* ws, char* out, f32x4 (&O)[5][3], bool xown) {
  const int lane = lane_id();
  const int hl = wv & 1, g = lane >> 4, m = lane & 15, b = 2 * c.bp + (wv >> 1);
  const u32 cu = blockIdx.x;
  const int sg = c.br * 8 + c.bp, me = c.rank * 4 + c.j;
  GAS u64* F3 = (GAS u64*)(ws + WS_F3) + sg * 256;
  const rsrc_t rm = mk(ws + WS_M, 1024u * 6144u);
  if (wv != c.rank) {
#pragma unroll
    for (int n = 0; n < 3; ++n)
#pragma unroll
      for (int c2 = 0; c2 < 2; ++c2) __builtin_amdgcn_raw_buffer_store_b128(cat(pk4(O[2 * c2][n]), pk4(O[2 * c2 + 1][n])), rm, lane * 16 + (n * 2 + c2) * 1024, (cu * 4 + wv) * 6144u, 16);
  }
  WAITV(0);
  STAMP(28);
  const u64 tok = *(LAS u64*)(sm + MISC + m * 8);
  {
    u32 old = 0;
    if (lane == 0) old = __hip_atomic_fetch_add((LAS u32*)(sm + MISC + 128), 1u, __ATOMIC_RELAXED, __HIP_MEMORY_SCOPE_WORKGROUP);
    old = __builtin_amdgcn_readfirstlane(old);
    if (old == 3u && lane == me) st_agent(F3 + me * 16, tok);
  }
  if (wv != c.rank) return;
  {
    const bool need = m != me && ((m & 3) == c.j || (xown && (m & 3) == (c.j ^ 2)));
    u32 spins = 0;
    while (!__all(!need || ld_agent(F3 + m * 16) == tok) && ++spins < SPIN_CAP) __builtin_amdgcn_s_sleep(1);
  }
  STAMP(29);
  u32x4 in[3][6];
#pragma unroll
  for (int r = 0; r < 3; ++r) {
    const u32 pr = (u32)((c.rank + 1 + r) & 3), pcu = (cu & ~6u) | (pr << 1);
#pragma unroll
    for (int i = 0; i < 6; ++i) in[r][i] = __builtin_amdgcn_raw_buffer_load_b128(rm, lane * 16 + i * 1024, (pcu * 4 + wv) * 6144u, 16);
  }
  const rsrc_t ro = mk(out, 768 * 2304);
  const u32 orow = (u32)(b * 48 + m) * 2304u + c.br * 1152;
  if (xown) {
    const rsrc_t rxx = mk(ws + WS_X, 1024u * 2048u);
    u32x2 xin[7][3];
#pragma unroll
    for (int r = 0; r < 7; ++r) {
      const u32 pr = (u32)((c.rank + 1 + r) & 3), pcu = ((cu & ~6u) | (pr << 1)) ^ (r >= 3 ? 16u : 0u);
#pragma unroll
      for (int n = 0; n < 3; ++n) xin[r][n] = __builtin_amdgcn_raw_buffer_load_b64(rxx, lane * 8 + n * 512, (pcu * 4 + wv) * 2048u, 16);
    }
#pragma unroll
    for (int n = 0; n < 3; ++n) {
      f32x4 a = O[4][n];
#pragma unroll
      for (int r = 0; r < 7; ++r) a += (f32x4){bflo(xin[r][n][0]), bfhi(xin[r][n][0]), bflo(xin[r][n][1]), bfhi(xin[r][n][1])};
      __builtin_amdgcn_raw_buffer_store_b64(pk4(a), ro, orow + n * (16 * 2304) + (512 + 32 * (c.j & 1) + 16 * hl + 4 * g) * 2, 0, 0);
    }
  }
#pragma unroll
  for (int n = 0; n < 3; ++n)
#pragma unroll
    for (int c2 = 0; c2 < 2; ++c2) {
      f32x4 a = O[2 * c2][n], d = O[2 * c2 + 1][n];
#pragma unroll
      for (int r = 0; r < 3; ++r) {
        const u32x4 v = in[r][n * 2 + c2];
        a += (f32x4){bflo(v[0]), bfhi(v[0]), bflo(v[1]), bfhi(v[1])};
        d += (f32x4){bflo(v[2]), bfhi(v[2]), bflo(v[3]), bfhi(v[3])};
      }
      const u32x2 pa = pk4(a), pd = pk4(d);
      auto s0 = __builtin_amdgcn_permlane16_swap(pa[0], pd[0], false, false);
      auto s1 = __builtin_amdgcn_permlane16_swap(pa[1], pd[1], false, false);
      __builtin_amdgcn_raw_buffer_store_b128((u32x4){s0[0], s1[0], s0[1], s1[1]}, ro,
          orow + n * (16 * 2304) + (128 * c.j + 64 * hl + 32 * c2 + 16 * (g & 1) + 8 * (g >> 1)) * 2, 0, 0);
    }
  STAMP(32);
}

DI void compute(Ctx& c, LAS char* sm, int wv) {
  start(c, sm, lane_id(), wv);
  char* ws = c.ws;
  const int bl = wv >> 1, hl = wv & 1;
  const u32 cu = blockIdx.x;
  int lane = lane_id(), g = lane >> 4, m = lane & 15, q = m >> 2, p = lane & 3;
  u32 wbase = (8 * g + q) * 256 + 32 * (q | ((g & 1) << 2)) + 8 * p;
  u32 xbase = (bl * 48 + m) * 128 + ((g ^ (m >> 1)) * 16);
  u32x4 Kf[3][4];
  f32x4 Q[8][3];
  u32x4 A1[8], B1[3];
  LAS char* aw[8]; LAS char* ax[2];
#pragma unroll
  for (int T = 0; T < 8; ++T) { A1[T] = (u32x4){0, 0, 0, 0}; PIN(A1[T]);
    aw[T] = vadd(sm + (wbase ^ (T << 5)), 12288 + hl * 16384);
#pragma unroll
    for (int n = 0; n < 3; ++n) { Q[T][n] = (f32x4){0.f, 0.f, 0.f, 0.f}; PIN(Q[T][n]); } }
#pragma unroll
  for (int n = 0; n < 3; ++n) { B1[n] = (u32x4){0, 0, 0, 0}; PIN(B1[n]); }
  ax[0] = vadd(sm + xbase, 0); ax[1] = vadd(sm + (xbase ^ 64), 0);
  SB();
  STAMP(33); WAITV(5); STAMP(34); BARRIER();
  STAMP(0);
#pragma unroll
  for (int kt = 0; kt < 3; ++kt) {
#pragma unroll 1
    for (int s = 0; s < 3; ++s) {
      if (s == 2) {
        const u32 l = lane_id(), kb = KS + wv * 4096 + (l & 15) * 256 + ((l >> 5) ^ (l & 15)) * 16 + ((l >> 1) & 8);
#pragma unroll
        for (int i = 0; i < 4; ++i) Kf[kt][i] = cat(lds64(sm + (kb ^ (i << 6))), lds64(sm + (kb ^ (i << 6) ^ 32)));
      }
      u32x4 A0[8], B0[3];
      half1<false>(Q, A1, B1, A0, B0, aw, ax, 0, 0);
      half1<true>(Q, A0, B0, A1, B1, aw, ax, 1, s == 2 ? -2 * QP : QP);
      if (kt == 0 && s == 0) WAITV(0);
      LBARRIER();
      STAMP(1 + 3 * kt + s);
    }
  }
  SB();
  lane = lane_id(); g = lane >> 4; m = lane & 15; q = m >> 2; p = lane & 3;
  wbase = (8 * g + q) * 256 + 32 * (q | ((g & 1) << 2)) + 8 * p;
  xbase = (bl * 48 + m) * 128 + ((g ^ (m >> 1)) * 16);
  A2 a;
#pragma unroll
  for (int ct = 0; ct < 4; ++ct) a.w[ct] = sm + (wbase ^ ((4 * hl + ct) << 5));
  a.e = sm + (8 * g + q) * 64 + 32 * (hl ^ (g & 1)) + 8 * p;
  a.v[0] = sm + xbase; a.v[1] = sm + (xbase ^ 64);
  G2 f0, f1;
  f32x4 O[5][3], U[3][5];
  {
    u32x4 qf[3][4];
    f32x4 S[3][3];
#pragma unroll
    for (int i = 0; i < 24; ++i) {
      Q[i & 7][i >> 3] = mfma(A1[i & 7], B1[i >> 3], Q[i & 7][i >> 3]);
      if (i >= 8) { const u32x2 t = pk4(Q[i & 7][(i >> 3) - 1]); qf[(i >> 3) - 1][(i & 7) >> 1][2 * (i & 1)] = t[0]; qf[(i >> 3) - 1][(i & 7) >> 1][2 * (i & 1) + 1] = t[1]; }
      SB();
    }
    STAMP(36);
#pragma unroll
    for (int i = 0; i < 36; ++i) {
      const int n = i / 12, s = (i % 12) / 3, kt = i % 3;
      S[n][kt] = mfma(Kf[kt][s], qf[n][s], s == 0 ? (f32x4){0.f, 0.f, 0.f, 0.f} : S[n][kt]);
      if (i >= 2 && i < 10) { const u32x2 t = pk4(Q[i - 2][2]); qf[2][(i - 2) >> 1][2 * (i & 1)] = t[0]; qf[2][(i - 2) >> 1][2 * (i & 1) + 1] = t[1]; }
      if (i >= 10 && i < 23) rd2<true>(i - 10, f0, a, 0, 0);
      if (i >= 23) rd2<true>(i - 23, f1, a, 0, 1);
      SB();
    }
    STAMP(37);
    const rsrc_t rp = mk(ws + WS_P, 256u * 18432u);
    const u32 pso = cu * 18432u + wv * 4608;
    LAS char* pl = vadd(sm + lane * 16, P_A + wv * 4608);
    LAS char* pl2 = vadd(sm + lane * 8, P_A + wv * 4608 + 3072);
    float mx[3], sum[3];
    int um = 0;
#define UM() do { if (um < 15) { U[um % 3][um / 3] = mfma(f0.v[um % 3], f0.w[um / 3], (f32x4){0.f, 0.f, 0.f, 0.f}); ++um; } SB(); } while (0)
#pragma unroll
    for (int n = 0; n < 3; ++n)
      mx[n] = max3(max3(S[n][0][0], S[n][0][1], S[n][0][2]), max3(S[n][0][3], S[n][1][0], S[n][1][1]),
                   max3(max3(S[n][1][2], S[n][1][3], S[n][2][0]), max3(S[n][2][1], S[n][2][2], S[n][2][3]), S[n][0][0]));
#pragma unroll
    for (int n = 0; n < 3; ++n) { auto r = __builtin_amdgcn_permlane16_swap(__float_as_uint(mx[n]), __float_as_uint(mx[n]), false, false); mx[n] = max2(__uint_as_float(r[0]), __uint_as_float(r[1])); }
#pragma unroll
    for (int n = 0; n < 3; ++n) { auto r = __builtin_amdgcn_permlane32_swap(__float_as_uint(mx[n]), __float_as_uint(mx[n]), false, false); mx[n] = max2(__uint_as_float(r[0]), __uint_as_float(r[1])) * -0.12751743344863963f; }
    SB();
#pragma unroll
    for (int n = 0; n < 3; ++n)
#pragma unroll
      for (int kt = 0; kt < 3; ++kt) {
#pragma unroll
        for (int i = 0; i < 4; ++i) S[n][kt][i] = __builtin_amdgcn_exp2f(__builtin_fmaf(S[n][kt][i], 0.12751743344863963f, mx[n]));
        UM();
      }
    STAMP(38);
#pragma unroll
    for (int n = 0; n < 3; ++n) { const f32x4 s4 = S[n][0] + S[n][1] + S[n][2]; sum[n] = (s4[0] + s4[1]) + (s4[2] + s4[3]); UM(); }
#pragma unroll
    for (int n = 0; n < 3; ++n) { auto r = __builtin_amdgcn_permlane16_swap(__float_as_uint(sum[n]), __float_as_uint(sum[n]), false, false); sum[n] = __uint_as_float(r[0]) + __uint_as_float(r[1]); }
#pragma unroll
    for (int n = 0; n < 3; ++n) { auto r = __builtin_amdgcn_permlane32_swap(__float_as_uint(sum[n]), __float_as_uint(sum[n]), false, false); sum[n] = __builtin_amdgcn_rcpf(__uint_as_float(r[0]) + __uint_as_float(r[1])); }
    SB();
    STAMP(39);
#pragma unroll
    for (int n = 0; n < 3; ++n) {
      const u32x4 pa = cat(pk4(S[n][0] * sum[n]), pk4(S[n][1] * sum[n]));
      const u32x2 pc = pk4(S[n][2] * sum[n]);
      __builtin_amdgcn_raw_buffer_store_b128(pa, rp, lane * 16 + n * 1024, pso, 16);
      __builtin_amdgcn_raw_buffer_store_b64(pc, rp, lane * 8 + 3072 + n * 512, pso, 16);
      *(LAS u32x4*)(pl + n * 1024) = pa;
      *(LAS u32x2*)(pl2 + n * 512) = pc;
      UM();
    }
  }
  STAMP(26);
  SB();
  LAS char* pp = sm + bl * 9216 + lane * 16;
  LAS char* pq = sm + bl * 9216 + lane * 8 + 3072;
  u32x4 UA[5]; u32x2 UC[5];
  const bool xown = (wv == c.rank) && ((c.j >> 1) == (c.rank & 1));
#define ENDI(gi) do { if ((gi) % 3 == 0) shift(a, RP); if ((gi) % 3 == 2) shift(a, -RP); if ((gi) + 1 == XB) WAITV(0); LBARRIER(); STAMP(1 + (gi)); SB(); } while (0)
#define SLOT(gi) ((gi) % 3 == 2 ? RP : 0)
#define BODY(t, PE, CE) do { \
    constexpr int g0 = 9 + 4 * (t); \
    if ((t) > 0) { \
      constexpr int pa_ = (t) == 1 ? P_A : (t) == 2 ? P_B : P_C; \
      finish2<PE, CE, true, (t) == 1>(O, U, UA, UC, f0, f1, a, SLOT(g0), vadd(pp, pa_), vadd(pq, pa_)); \
      if ((t) == 2 && !xown) { const rsrc_t rxx = mk(ws + WS_X, 1024u * 2048u); \
        _Pragma("unroll") for (int n = 0; n < 3; ++n) __builtin_amdgcn_raw_buffer_store_b64(pk4(O[4][n]), rxx, lane * 8 + n * 512, (cu * 4 + wv) * 2048u, 16); } \
      step2<CE, true, false, false, 0>(U, f0, f1, a, 0, 0, UA, UC); } \
    ENDI(g0); \
    step2<CE, false, true, false, 0>(U, f1, f0, a, SLOT(g0 + 1), 0, UA, UC); \
    step2<CE, false, true, false, 3>(U, f0, f1, a, SLOT(g0 + 1), 1, UA, UC); ENDI(g0 + 1); \
    step2<CE, false, true, false, 0>(U, f1, f0, a, SLOT(g0 + 2), 0, UA, UC); \
    step2<CE, true, true, true, 3>(U, f0, f1, a, SLOT(g0 + 2), 1, UA, UC); ENDI(g0 + 2); \
    step2<CE, false, true, false, 0>(U, f1, f0, a, SLOT(g0 + 3), 0, UA, UC); \
    step2<CE, false, true, false, 3>(U, f0, f1, a, SLOT(g0 + 3), 1, UA, UC); ENDI(g0 + 3); \
  } while (0)
  BODY(0, true, true);
  BODY(1, true, true);
  BODY(2, true, false);
  BODY(3, false, false);
  finish2<false, false, false, false>(O, U, UA, UC, f0, f1, a, 0, vadd(pp, P_A), vadd(pq, P_A));
  STAMP(27);
  tail(c, sm, wv, ws, c.out, O, xown);
}

__global__ __launch_bounds__(512, 2) void fused_attn_block(const char* xA, const char* kA, const char* vA, const char* wqA, const char* woA,
                                                          const char* xB, const char* kB, const char* vB, const char* wqB, const char* woB,
                                                          char* out, char* ws) {
  __shared__ __attribute__((aligned(256))) char smem[LDS_BYTES];
  LAS char* sm = (LAS char*)smem;
  const int wv = __builtin_amdgcn_readfirstlane(threadIdx.x >> 6);
  Ctx c;
  c.stamps = 0; c.ta = 0; c.tb = 0;
  STAMP(63);
  const int id = blockIdx.x;
  c.br = id & 1; c.rank = (id >> 1) & 3; c.j = (id >> 3) & 3; c.bp = id >> 5;
  c.h0 = c.rank * 8; c.kvsh = 1 - c.br;
  {
    const u64 ka = (u64)(uintptr_t)__builtin_amdgcn_kernarg_segment_ptr();
    asm volatile("s_load_dwordx2 %0, %8, 0x0\n\ts_load_dwordx2 %1, %8, 0x18\n\ts_load_dwordx2 %2, %8, 0x8\n\ts_load_dwordx2 %3, %8, 0x10\n\t"
                 "s_load_dwordx2 %4, %8, 0x20\n\ts_load_dwordx2 %5, %9, 0x50\n\ts_load_dwordx2 %6, %9, 0x58\n\ts_getpc_b64 %7"
                 : "=&s"(c.a[0]), "=&s"(c.a[1]), "=&s"(c.a[2]), "=&s"(c.a[3]), "=&s"(c.a[4]), "=&s"(c.a[5]), "=&s"(c.a[6]), "=&s"(c.pc)
                 : "s"(ka + c.br * 40), "s"(ka) : "memory");
  }
  if (wv == 4) loader<0>(c, sm);
  else if (wv == 5) loader<1>(c, sm);
  else if (wv == 6) loader<2>(c, sm);
  else if (wv == 7) comm(c, sm);
  else if (ABL_NOCOMP) { start(c, sm, lane_id(), wv); STAMP(33); WAITV(5); STAMP(34); BARRIER(); STAMP(0); WAITV(0); for (int g = 1; g < 26; ++g) { BARRIER(); STAMP(g); } }
  else compute(c, sm, wv);
}

template <int MODE> __global__ __launch_bounds__(64) void probe_k(const char* kB, const char* wqB, const char* woB) {
  __shared__ __attribute__((aligned(16))) char sm_[2048];
  const int lane = threadIdx.x, wg = blockIdx.x;
  rsrc_t r; u32 voff, so, step;
  if (MODE == 0) { r = mk(kB, 16 * 32 * 48 * 256); voff = lane * 16; so = wg * 24576; step = 1024; }
  else if (MODE == 1) { r = mk(wqB, 576 * 8192); voff = (lane >> 4) * 8192 + (lane & 15) * 16; so = (wg & 31) * 256 + (wg >> 5) * (72 * 8192); step = 4 * 8192; }
  else { r = mk(woB, 4096 * 1152); voff = (lane >> 4) * 1152 + (lane & 15) * 16; so = (wg & 3) * 256 + (wg >> 2) * (64 * 1152); step = 4 * 1152; }
  const u64 t0 = __builtin_amdgcn_s_memrealtime();
  for (int i = 0; i < 16; ++i) { dma(r, (LAS char*)sm_ + (i & 1) * 1024, voff, so); so += step; WAITV(0); }
  const u64 t1 = __builtin_amdgcn_s_memrealtime();
  const u64 d = (t1 - t0) * 20u; while (__builtin_amdgcn_s_memrealtime() - t1 < d) __builtin_amdgcn_s_sleep(4);
}
extern "C" void kernel_launch(void* const* d_in, const int* in_sizes, int n_in, void* d_out, int out_size, void* d_ws, size_t ws_size,
                              hipStream_t stream) {
  const char* const* in = (const char* const*)d_in;
  probe_k<0><<<256, 64, 0, stream>>>(in[6], in[8], in[9]);
  fused_attn_block<<<256, 512, 0, stream>>>(in[0], in[1], in[2], in[3], in[4], in[5], in[6], in[7], in[8], in[9], (char*)d_out, (char*)d_ws);
}
```

```cpp
#include <hip/hip_runtime.h>
#include <stdint.h>

typedef unsigned u32;
typedef unsigned long long u64;
typedef u32 u32x4 __attribute__((ext_vector_type(4)));
typedef u32 u32x2 __attribute__((ext_vector_type(2)));
typedef u32 u32x8 __attribute__((ext_vector_type(8)));
typedef float f32x4 __attribute__((ext_vector_type(4)));
typedef float f32x2 __attribute__((ext_vector_type(2)));
typedef short s16x4 __attribute__((ext_vector_type(4)));
typedef __bf16 bf16x8 __attribute__((ext_vector_type(8)));
typedef __bf16 bf16x2 __attribute__((ext_vector_type(2)));
#define LAS __attribute__((address_space(3)))
#define GAS __attribute__((address_space(1)))
#define DI __device__ __forceinline__
typedef __amdgpu_buffer_rsrc_t rsrc_t;

#define DIAG 0
#define CODEPF 32
#define XB 11
#define ROTH 1
#define ROTK 4
#define PDMA 0
#define H3LATE 0
#define HPRIO 0
#define KPOS 0
#define NODRY 1
#define ABL_NOLOAD 0
#define ABL_NOCOMP 0

constexpr int QP = 45056;
constexpr int RP = 32768;
constexpr int KS = 135168;
constexpr int P_A = 131072;
constexpr int XL = 149504;
constexpr int P_B2 = 155904;
constexpr int OWN = 0;
constexpr int MISC = 153600;
constexpr int JUNK = 153856;
constexpr int TOKL = 154880;
constexpr int LDS_BYTES = 157952;
constexpr u32 WS_F1 = 0;
constexpr u32 WS_F3 = 32768;
constexpr u32 WS_R = 65536;
constexpr u32 WS_P = 131072;
constexpr u32 WS_M = WS_P + 256u * 18432u;
constexpr u32 WS_X = WS_M + 1024u * 6144u;
constexpr u32 WS_DIAG = 16u << 20;
constexpr u64 TOKK = 0x9E3779B97F4A7C15ull;
constexpr u32 SPIN_CAP = 1u << 20;

DI rsrc_t mk(const void* p, u32 bytes) { return __builtin_amdgcn_make_buffer_rsrc((void*)p, 0, bytes, 0x00020000); }
DI void dma(rsrc_t r, LAS char* dst, u32 voff, u32 soff) { if (ABL_NOLOAD) return; __builtin_amdgcn_raw_ptr_buffer_load_lds(r, (LAS void*)dst, 16, voff, soff, 0, 0); }
DI void dma_asm(rsrc_t r, u32 dst, u32 voff, u32 soff) {
  if (ABL_NOLOAD) return;
  asm volatile("s_mov_b32 m0, %0\n\ts_nop 0\n\tbuffer_load_dwordx4 %1, %2, %3 offen lds" :: "s"(dst), "v"(voff), "s"(r), "s"(soff) : "memory", "m0");
}
template <int N, int STEP> DI void dma_run(rsrc_t r, u32 dst, u32 voff, u32 soff) {
  if (ABL_NOLOAD) return;
  u32 t;
#define PIECE_ "s_add_u32 m0, m0, 0x1000\n\ts_add_u32 %0, %0, %5\n\tbuffer_load_dwordx4 %2, %3, %0 offen lds\n\t"
#define FIRST_ "s_mov_b32 m0, %1\n\ts_mov_b32 %0, %4\n\tbuffer_load_dwordx4 %2, %3, %0 offen lds\n\t"
  if (N == 2) asm volatile(FIRST_ PIECE_ : "=&s"(t) : "s"(dst), "v"(voff), "s"(r), "s"(soff), "n"(STEP) : "memory", "m0", "scc");
  if (N == 3) asm volatile(FIRST_ PIECE_ PIECE_ : "=&s"(t) : "s"(dst), "v"(voff), "s"(r), "s"(soff), "n"(STEP) : "memory", "m0", "scc");
  if (N == 4) asm volatile(FIRST_ PIECE_ PIECE_ PIECE_ : "=&s"(t) : "s"(dst), "v"(voff), "s"(r), "s"(soff), "n"(STEP) : "memory", "m0", "scc");
}
#define WAITV(n) asm volatile("s_waitcnt vmcnt(%0)" ::"i"(n) : "memory")
#define BARRIER() asm volatile("s_barrier" ::: "memory")
#define LBARRIER() asm volatile("s_waitcnt lgkmcnt(0)\n\ts_barrier" ::: "memory")
#define SB() __builtin_amdgcn_sched_barrier(0)
#define PIN(x) asm volatile("" : "+v"(x))
DI int lane_id() { int l = __builtin_amdgcn_mbcnt_hi(~0u, __builtin_amdgcn_mbcnt_lo(~0u, 0u)); PIN(l); return l; }
DI u32 vmcnt_now() { u32 v; asm volatile("s_getreg_b32 %0, hwreg(HW_REG_IB_STS)" : "=s"(v) :: "memory"); return v & 0x00C0000Fu; }
DI f32x4 mfma(u32x4 a, u32x4 b, f32x4 c) { return __builtin_amdgcn_mfma_f32_16x16x32_bf16(__builtin_bit_cast(bf16x8, a), __builtin_bit_cast(bf16x8, b), c, 0, 0, 0); }
DI u32 pk(float a, float b) { f32x2 f = {a, b}; return __builtin_bit_cast(u32, __builtin_convertvector(f, bf16x2)); }
DI u32x2 pk4(f32x4 v) { u32x2 r = {pk(v[0], v[1]), pk(v[2], v[3])}; return r; }
DI u32x4 cat(u32x2 a, u32x2 b) { u32x4 r = {a[0], a[1], b[0], b[1]}; return r; }
DI u32x2 trd(LAS char* p) { return __builtin_bit_cast(u32x2, __builtin_amdgcn_ds_read_tr16_b64_v4i16((LAS s16x4*)p)); }
DI u32x4 lds128(LAS char* p) { return *(LAS u32x4*)p; }
DI u32x2 lds64(LAS char* p) { return *(LAS u32x2*)p; }
DI float bflo(u32 x) { return __builtin_bit_cast(float, x << 16); }
DI float bfhi(u32 x) { return __builtin_bit_cast(float, x & 0xffff0000u); }
DI u64 ld_agent(GAS u64* p) { return __hip_atomic_load(p, __ATOMIC_RELAXED, __HIP_MEMORY_SCOPE_AGENT); }
DI void st_agent(GAS u64* p, u64 v) { __hip_atomic_store(p, v, __ATOMIC_RELAXED, __HIP_MEMORY_SCOPE_AGENT); }
DI u64 rdlane64(u64 v, int l) { return ((u64)(u32)__builtin_amdgcn_readlane((int)(v >> 32), l) << 32) | (u32)__builtin_amdgcn_readlane((int)v, l); }
DI LAS char* vadd(LAS char* p, int d) { u32 r; asm volatile("v_add_u32 %0, %1, %2" : "=v"(r) : "s"(d), "v"((u32)(uintptr_t)p)); return (LAS char*)(uintptr_t)r; }
DI int wlane(int val, int idx, int old) { asm volatile("s_mov_b32 m0, %2\n\ts_nop 0\n\tv_writelane_b32 %0, %1, m0" : "+v"(old) : "s"(val), "s"(idx) : "m0"); return old; }
#define STAMP(i) do {} while (0)

struct Ctx {
  const char *x, *k, *v, *wq, *wo;
  u64 pc;
  char *out, *ws;
  int stamps; u32 ta, tb;
  int br, rank, bp, j, h0, kvsh, hp, rot, kpos; u64 ord;
  int dummy_;
};

DI void start(Ctx& c, LAS char* sm, int lane, int wv) {
  const rsrc_t rx = mk(c.x, 768 * 1152), rq = mk(c.wq, 576 * 8192);
  const u32 l4 = lane >> 4;
  const u32 vx = (lane >> 3) * 1152 + ((lane & 7) ^ (l4 | ((wv & 1) << 2))) * 16;
  const u32 vq = l4 * 8192 + ((lane & 15) ^ ((l4 | ((wv & 2) << 1)) << 1)) * 16;
  const u32 k0 = c.rot;
  const u32 xs = (u32)(c.bp * 96 + wv * 8) * 1152u + k0 * 128, qs = (u32)(c.h0 + 2 * c.hp) * 256u + wv * (4 * 8192) + k0 * (64 * 8192);
  const u32 d = (u32)(uintptr_t)sm + wv * 1024;
  dma_asm(rx, d, vx, xs);
#pragma unroll
  for (int i = 0; i < 4; ++i) dma_asm(rq, d + 12288 + i * 8192, vq, qs + (i & 1) * (32 * 8192) + (i >> 1) * 256);
  dma_asm(rx, wv < 4 ? d + 8192 : (u32)(uintptr_t)sm + JUNK, vx, wv < 4 ? xs + 64 * 1152 : 0x7f000000u);
  STAMP(30);
}

DI u64 ld_async(const char* base, u32 off) { u64 r; asm volatile("global_load_dwordx2 %0, %1, %2 sc1" : "=v"(r) : "v"(off), "s"(base) : "memory"); return r; }
DI u64 ld_now(const char* base, u32 off) { u64 r; asm volatile("global_load_dwordx2 %0, %1, %2 sc1\n\ts_waitcnt vmcnt(0)" : "=&v"(r) : "v"(off), "s"(base) : "memory"); return r; }
DI void st_one(const char* base, u32 off, u64 v, u64 lanes) {
  asm volatile("s_mov_b64 exec, %3\n\tglobal_store_dwordx2 %0, %1, %2 sc1\n\ts_mov_b64 exec, -1" :: "v"(off), "v"(v), "s"(base), "s"(lanes) : "memory");
}

DI u64 helper1(Ctx& c, LAS char* sm, int l) {
  const int lane = lane_id(), hkv = 32 >> c.kvsh;
  const u32 base = (u32)(uintptr_t)sm;
  const rsrc_t rx = mk(c.x, 768 * 1152), rq = mk(c.wq, 576 * 8192), rk = mk(c.k, (u32)(16 * hkv) * (48 * 256));
  const u32 l4 = lane >> 4;
  const u32 vx = (lane >> 3) * 1152 + ((lane & 7) ^ (l4 | ((l & 1) << 2))) * 16;
  const u32 vq = l4 * 8192 + ((lane & 15) ^ ((l4 | ((l & 2) << 1)) << 1)) * 16;
  const u32 vk = l4 * 256 + ((lane & 15) ^ (l4 | (l << 2))) * 16;
  const u32 xs = (u32)(c.bp * 96 + l * 8) * 1152u, qs = (u32)(c.h0 + 2 * c.hp) * 256u + l * (4 * 8192);
  const u32 k0 = (u32)(2 * c.bp * hkv + ((c.h0 + 2 * c.hp) >> c.kvsh)) * (48 * 256) + l * 1024, k1 = c.kvsh ? 0u : 48u * 256u, k2 = (u32)hkv * (48 * 256);
  const int sg = c.br * 8 + c.bp, me = c.rank * 4 + c.j, m = lane & 15;
  u64 tok = 0;
  if (HPRIO) __builtin_amdgcn_s_setprio(HPRIO);
  STAMP(33);
  int slot = 1, blk = 0;
#pragma unroll 1
  for (int st = 1; st < 9; ++st) {
    const u32 k = ROTK == 4 ? (u32)(c.ord >> (4 * st)) & 15u : ROTK == 1 ? blk + (slot + c.rot >= 3 ? slot + c.rot - 3 : slot + c.rot) : (st + c.rot >= 9 ? st + c.rot - 9 : st + c.rot), d = base + slot * QP + l * 1024;
    dma_run<3, 32 * 1152>(rx, d, vx, xs + k * 128);
    dma_run<4, 16 * 8192>(rq, d + 12288, vq, qs + k * (64 * 8192));
    dma_run<4, 16 * 8192>(rq, d + 28672, vq, qs + k * (64 * 8192) + 256);
    if (slot == c.kpos) {
#pragma unroll
      for (int i = 0; i < 4; ++i) dma_asm(rk, base + KS + l * 1024 + i * 4096, vk, k0 + (i & 1) * k1 + (i >> 1) * k2 + (blk / 3) * 4096);
    }
    if (st == 1) {
      u32 line = ((((blockIdx.x >> 3) & 1) * 4 + l) * 64 + lane); line = line < CODEPF * 8 ? line : 0u;
      asm volatile("s_mov_b32 m0, %0\n\ts_nop 0\n\tglobal_load_lds_dword %1, %2" :: "s"(base + JUNK), "v"(line * 128), "s"(c.pc & ~255ull) : "memory", "m0");
      asm volatile("s_mov_b32 m0, %0\n\ts_nop 0\n\tglobal_load_lds_dwordx4 %1, %2 sc1" :: "s"(base + TOKL), "v"(m * 128), "s"(c.ws + WS_F1 + sg * 2048) : "memory", "m0");
      if (c.kpos == 1) WAITV(17); else WAITV(13);
      STAMP(34);
    } else if (slot == c.kpos) WAITV(15); else WAITV(11);
    if (slot == 2) { slot = 0; blk += 3; } else ++slot;
    BARRIER();
    if (st == 3) {
      tok = *(LAS u64*)(sm + TOKL + m * 16) + TOKK;
      if (l == 3) {
        st_one(c.ws + WS_R + sg * 2048, m * 128 + me * 8, tok, 0xffffull);
        *(LAS u64*)(sm + MISC + m * 8) = tok; *(LAS u32*)(sm + MISC + 128) = 0u;
        asm volatile("s_waitcnt lgkmcnt(0)" ::: "memory");
      }
    }
  }
  return tok;
}

struct H2 {
  rsrc_t ro, rv;
  u32 vo, vv, ve, oso, eso, vs[3], base;
  int hown, h0, kvsh;
  template <bool EXT> DI void p2(int st) {
    const int h = h0 + ((hown + (st >> 1)) & 7);
    const u32 row = (u32)(h * 128 + (st & 1) * 64) * 1152u, vh = (u32)(h >> kvsh) * (48 * 256) + (st & 1) * 128, d = base + (st & 3) * RP;
#pragma unroll
    for (int i = 0; i < 3; ++i) dma_asm(rv, d + 16384 + i * 4096, vv, vs[i] + vh);
    dma_run<4, 16 * 1152>(ro, d, vo, oso + row);
    if (EXT) dma_asm(ro, d + 28672, ve, eso + row);
  }
};

DI void helper2(Ctx& c, LAS char* sm, int l, u64 tok) {
  const int lane = lane_id(), hkv = 32 >> c.kvsh, l4 = lane >> 4;
  H2 z;
  z.ro = mk(c.wo, 4096 * 1152); z.rv = mk(c.v, (u32)(16 * hkv) * (48 * 256));
  z.vo = l4 * 1152 + ((lane & 15) ^ ((l4 | ((l & 2) << 1)) << 1)) * 16;
  z.vv = (lane >> 3) * 256 + ((lane & 7) ^ (l4 | ((l & 1) << 2))) * 16;
  z.ve = (lane >> 2) * 1152 + ((lane & 3) ^ ((lane >> 5) << 1)) * 16;
  z.oso = c.j * 256 + l * (4 * 1152); z.eso = 1024 + (c.j & 1) * 64 + l * (16 * 1152);
  { const u32 b0 = (u32)(2 * c.bp * hkv) * (48 * 256), b1 = b0 + (u32)hkv * (48 * 256);
    z.vs[0] = b0 + l * 2048; z.vs[1] = l < 2 ? b0 + (l + 4) * 2048 : b1 + (l - 2) * 2048; z.vs[2] = b1 + (l + 2) * 2048; }
  z.base = (u32)(uintptr_t)sm + l * 1024; z.hown = 2 * c.hp; z.h0 = c.h0; z.kvsh = c.kvsh;
  const int sg = c.br * 8 + c.bp, me = c.rank * 4 + c.j;
  const char* F1 = c.ws + WS_F1 + sg * 2048;
  z.p2<true>(0); WAITV(8); BARRIER();
  z.p2<true>(1); WAITV(8); BARRIER();
  const u64 mytok = rdlane64(tok, me);
  const rsrc_t rp = mk(c.ws + WS_P, 256u * 18432u);
  const u32 p4 = 16384 + (l & 1) * 1024;
  LAS char* pl = sm + P_A + l * 1024 + lane * 16;
  LAS char* pl4 = sm + P_A + p4 + lane * 16;
  LAS char* pm = sm + 28672 + l * 1024 + lane * 16;
  LAS char* pm4 = sm + P_B2 + (l & 1) * 1024 + lane * 16;
  const int g = lane >> 4, m = lane & 15, hl = c.rank & 1;
  const bool xown = (c.j >> 1) == hl;
  const u32 cu = blockIdx.x, orow = (u32)((2 * c.bp + (c.rank >> 1)) * 48 + m) * 2304u + c.br * 1152;
  const bool need = m != me && (l < 3 ? (m & 3) == c.j : ((m ^ c.j) & 1) == 0);
  {
    u64 r = ld_async(c.ws + WS_R + sg * 2048, me * 128 + m * 8);
    z.p2<true>(2); z.p2<true>(3); WAITV(16); PIN(r);
    if (!__all(r == mytok))
      for (u32 spins = 0; spins < SPIN_CAP; ++spins) if (__all(ld_now(c.ws + WS_R + sg * 2048, me * 128 + m * 8) == mytok)) break;
  }
  BARRIER();
  z.p2<true>(4); WAITV(16); BARRIER();
  if (XB == 11 && l == 3) st_one(F1, me * 128, mytok, 1ull);
  z.p2<true>(5); WAITV(16); BARRIER();
  if (XB == 12 && l == 3) st_one(F1, me * 128, mytok, 1ull);
  STAMP(40);
  z.p2<true>(6); WAITV(16); BARRIER();
#define PLOAD(pv, t, i) asm volatile("buffer_load_dwordx4 %0, %1, %2, %3 offen sc1" : "=v"(pv[i]) : "v"(lane * 16), "s"(rp), \
    "s"((u32)((c.bp * 4 + ((c.j + (t)) & 3)) * 8 + c.rank * 2 + c.br) * 18432u + ((i) < 4 ? l * 1024 + (i) * 4096 : p4)) : "memory")
#define PUTA(pv, i) do { PIN(pv[i]); *(LAS u32x4*)((i) < 4 ? pl + (i) * 4096 : pl4) = pv[i]; } while (0)
#define PUTB(pv, i) do { PIN(pv[i]); *(LAS u32x4*)((i) < 2 ? pm + (i) * RP : (i) < 4 ? pm + 2 * RP + ((i) - 2) * RP : pm4) = pv[i]; } while (0)
  u32x4 pa[5], pb[5], pc[5];
  {
    const u64 want = __shfl(tok, c.rank * 4 + (lane & 3));
    u64 r = ld_async(F1, (c.rank * 4 + (lane & 3)) * 128);
    z.p2<true>(7); WAITV(8); PIN(r);
    if (!__all(r == want))
      for (u32 spins = 0; spins < SPIN_CAP; ++spins) if (__all(ld_now(F1, (c.rank * 4 + (lane & 3)) * 128) == want)) break;
  }
  STAMP(41);
  PLOAD(pa, 1, 0); PLOAD(pa, 1, 1); PLOAD(pa, 1, 2); PLOAD(pa, 1, 3); PLOAD(pa, 1, 4); WAITV(13); BARRIER();
  z.p2<false>(8); WAITV(7); PUTA(pa, 0); BARRIER();
  PUTA(pa, 1); PUTA(pa, 2); z.p2<false>(9); PLOAD(pb, 2, 0); PLOAD(pb, 2, 1); BARRIER();
  PUTA(pa, 3); PUTA(pa, 4); z.p2<false>(10); PLOAD(pb, 2, 2); WAITV(17); LBARRIER(); STAMP(42);
  PLOAD(pb, 2, 3); PLOAD(pb, 2, 4); z.p2<false>(11); WAITV(7);
  PUTB(pb, 0); PUTB(pb, 1); PUTB(pb, 2); PUTB(pb, 3); PUTB(pb, 4); BARRIER(); STAMP(44);
  z.p2<false>(12); PLOAD(pc, 3, 0); PLOAD(pc, 3, 1); BARRIER();
  z.p2<false>(13); PLOAD(pc, 3, 2); WAITV(17); BARRIER();
  if (l == 3) st_one(c.ws + WS_F3 + sg * 2048, me * 128 + 8, mytok, 1ull);
  z.p2<false>(14); PLOAD(pc, 3, 3); PLOAD(pc, 3, 4); WAITV(19); LBARRIER();
  z.p2<false>(15); WAITV(7);
  PUTA(pc, 0); PUTA(pc, 1); PUTA(pc, 2); PUTA(pc, 3); PUTA(pc, 4); WAITV(0); LBARRIER(); STAMP(46);
#pragma unroll 1
  for (int pass = NODRY; pass < 2; ++pass) {
    const u32 on = pass ? ~0u : 0u;
    const rsrc_t ro = mk(c.out, (768 * 2304) & on);
    if (pass) {
      BARRIER();
      if (l == 3 && !xown) break;
      for (u32 spins = 0; spins < SPIN_CAP; ++spins) if (__all(!need || ld_now(c.ws + WS_F3 + sg * 2048, m * 128 + (l == 3 ? 8 : 0)) == tok)) break;
      STAMP(29);
    }
    if (l < 3) {
      const rsrc_t rm = mk(c.ws + WS_M, (1024u * 6144u) & on);
      u32x4 in[3][2];
#pragma unroll
      for (int r = 0; r < 3; ++r) {
        const u32 pcu = (cu & ~6u) | (((c.rank + 1 + r) & 3) << 1);
#pragma unroll
        for (int i = 0; i < 2; ++i) in[r][i] = __builtin_amdgcn_raw_buffer_load_b128(rm, lane * 16 + i * 1024, (pcu * 4 + c.rank) * 6144u + l * 2048, 16);
      }
#pragma unroll
      for (int c2 = 0; c2 < 2; ++c2) {
        f32x4 a = *(LAS f32x4*)(sm + OWN + (l * 4 + 2 * c2) * 1024 + lane * 16), d = *(LAS f32x4*)(sm + OWN + (l * 4 + 2 * c2 + 1) * 1024 + lane * 16);
#pragma unroll
        for (int r = 0; r < 3; ++r) {
          const u32x4 v = in[r][c2];
          a += (f32x4){bflo(v[0]), bfhi(v[0]), bflo(v[1]), bfhi(v[1])};
          d += (f32x4){bflo(v[2]), bfhi(v[2]), bflo(v[3]), bfhi(v[3])};
        }
        const u32x2 pa = pk4(a), pd = pk4(d);
        auto s0 = __builtin_amdgcn_permlane16_swap(pa[0], pd[0], false, false);
        auto s1 = __builtin_amdgcn_permlane16_swap(pa[1], pd[1], false, false);
        __builtin_amdgcn_raw_buffer_store_b128((u32x4){s0[0], s1[0], s0[1], s1[1]}, ro,
            orow + (128 * c.j + 64 * hl + 32 * c2 + 16 * (g & 1) + 8 * (g >> 1)) * 2, l * (16 * 2304), 0);
      }
    } else {
      const rsrc_t rxx = mk(c.ws + WS_X, (1024u * 2048u) & on);
      u32x2 xin[7][3];
#pragma unroll
      for (int r = 0; r < 7; ++r) {
        const u32 pcu = ((cu & ~6u) | (((c.rank + 1 + r) & 3) << 1)) ^ (r >= 3 ? 16u : 0u);
#pragma unroll
        for (int n = 0; n < 3; ++n) xin[r][n] = __builtin_amdgcn_raw_buffer_load_b64(rxx, lane * 8 + n * 512, (pcu * 4 + c.rank) * 2048u, 16);
      }
#pragma unroll
      for (int n = 0; n < 3; ++n) {
        f32x4 a = *(LAS f32x4*)(sm + XL + n * 1024 + lane * 16);
#pragma unroll
        for (int r = 0; r < 7; ++r) a += (f32x4){bflo(xin[r][n][0]), bfhi(xin[r][n][0]), bflo(xin[r][n][1]), bfhi(xin[r][n][1])};
        __builtin_amdgcn_raw_buffer_store_b64(pk4(a), ro, orow + n * (16 * 2304) + (512 + 32 * (c.j & 1) + 16 * hl + 4 * g) * 2, 0, 0);
      }
    }
  }
  STAMP(32);
}

template <bool ADV, int I0, int I1> DI void half1(f32x4 (&Q)[8][3], const u32x4 (&A)[8], const u32x4 (&B)[3], u32x4 (&An)[8], u32x4 (&Bn)[3],
                                  LAS char* (&w)[8], LAS char* (&x)[2], int u, int adv) {
  u32x2 lo[8];
#pragma unroll
  for (int i = I0; i < I1; ++i) {
    Q[i / 3][i % 3] = mfma(A[i / 3], B[i % 3], Q[i / 3][i % 3]);
    constexpr int what[19] = {70, 71, 00, 01, 100, 10, 11, 101, 20, 21, 102, 30, 31, 60, 40, 41, 61, 50, 51};
    if (i < 19) {
      const int k = what[i];
      if (k >= 100) Bn[k - 100] = lds128(x[u] + (k - 100) * 2048);
      else if (k % 10 == 0) lo[k / 10] = trd(w[k / 10] + u * 8192);
      else An[k / 10] = cat(lo[k / 10], trd(w[k / 10] + u * 8192 + 1024));
    } else if (ADV) {
      if (i < 23) { w[2 * (i - 19)] = vadd(w[2 * (i - 19)], adv); w[2 * (i - 19) + 1] = vadd(w[2 * (i - 19) + 1], adv); }
      else { x[0] = vadd(x[0], adv); x[1] = vadd(x[1], adv); }
    }
    SB();
  }
}
struct G2 { u32x4 v[3], w[5]; u32x2 lo; };
struct A2 { LAS char* w[4]; LAS char* e; LAS char* v[2]; };
DI void shift(A2& a, int d) {
#pragma unroll
  for (int i = 0; i < 4; ++i) a.w[i] = vadd(a.w[i], d);
  a.e = vadd(a.e, d); a.v[0] = vadd(a.v[0], d); a.v[1] = vadd(a.v[1], d);
}
template <bool EXT> DI void rd2(int i, G2& f, const A2& a, int off, int u) {
  if (i < 3) f.v[i] = lds128(a.v[u] + off + 16384 + i * 2048);
  else if (i < 11) { const int ct = (i - 3) >> 1; if ((i - 3) & 1) f.w[ct] = cat(f.lo, trd(a.w[ct] + off + u * 8192 + 1024)); else f.lo = trd(a.w[ct] + off + u * 8192); }
  else if (EXT && i == 11) f.lo = trd(a.e + off + 28672 + u * 2048);
  else if (EXT && i == 12) f.w[4] = cat(f.lo, trd(a.e + off + 28672 + u * 2048 + 256));
}
template <bool E, bool FIRST, bool READ, bool PACK, int DBL>
DI void step2(f32x4 (&U)[3][5], const G2& f, G2& fn, const A2& a, int off, int u, u32x4 (&UA)[5], u32x2 (&UC)[5]) {
  const f32x4 z = {0.f, 0.f, 0.f, 0.f};
#pragma unroll
  for (int i = 0; i < (E ? 15 : 12); ++i) {
    const int ct = i / 3, kt = i % 3;
    if (PACK) { const u32x2 t = pk4(U[kt][ct]); if (kt == 0) { UA[ct][0] = t[0]; UA[ct][1] = t[1]; } else if (kt == 1) { UA[ct][2] = t[0]; UA[ct][3] = t[1]; } else UC[ct] = t; SB(); }
    U[kt][ct] = mfma(f.v[kt], f.w[ct], FIRST ? z : U[kt][ct]);
    if (READ) { if (i < DBL) { rd2<E>(2 * i, fn, a, off, u); rd2<E>(2 * i + 1, fn, a, off, u); } else rd2<E>(i + DBL, fn, a, off, u); }
    SB();
  }
}
template <bool PE, bool CE, bool READ, bool FIRST, int BS>
DI void finish2(f32x4 (&O)[5][3], f32x4 (&U)[3][5], const u32x4 (&A0)[5], const u32x2 (&C0)[5], G2& f0, G2& f1, const A2& a, int off, LAS char* p, LAS char* pc, LAS char* pc2) {
  constexpr int N = PE ? 15 : 12;
  u32x4 pa0[3], pa1[3], pcc[3], A1[5]; u32x2 C1[5], lo[3];
#pragma unroll
  for (int i = 0; i < N; ++i) {
    U[i % 3][i / 3] = mfma(f1.v[i % 3], f1.w[i / 3], U[i % 3][i / 3]);
    if (i < 3) pa0[i] = lds128(p + i * 1024);
    else if (i < 6) pa1[i - 3] = lds128(p + BS + (i - 3) * 1024);
    else if (READ) rd2<CE>(i - 6, f0, a, off, 0);
    SB();
  }
#pragma unroll
  for (int i = 0; i < N; ++i) {
    const int ct = i / 3, n = i % 3;
    O[ct][n] = mfma(A0[ct], pa0[n], FIRST ? (f32x4){0.f, 0.f, 0.f, 0.f} : O[ct][n]);
    const u32x2 t = pk4(U[n][ct]);
    if (n == 0) { A1[ct][0] = t[0]; A1[ct][1] = t[1]; } else if (n == 1) { A1[ct][2] = t[0]; A1[ct][3] = t[1]; } else C1[ct] = t;
    SB();
  }
#pragma unroll
  for (int i = 0; i < N; ++i) {
    O[i / 3][i % 3] = mfma(A1[i / 3], pa1[i % 3], O[i / 3][i % 3]);
    if (i < 3) lo[i] = lds64(i < 2 ? pc + i * 512 : pc2);
    else if (i < 6) pcc[i - 3] = cat(lo[i - 3], lds64(i < 5 ? pc + BS + (i - 3) * 512 : pc2 + 512));
    else if (READ) rd2<CE>(i + N - 12, f0, a, off, 0);
    SB();
  }
  if (READ && N == 12) rd2<CE>(12, f0, a, off, 0);
#pragma unroll
  for (int i = 0; i < N; ++i) {
    O[i / 3][i % 3] = mfma(cat(C0[i / 3], C1[i / 3]), pcc[i % 3], O[i / 3][i % 3]);
    if (READ) rd2<CE>(i, f1, a, off, 1);
    SB();
  }
  if (READ && N == 12) rd2<CE>(12, f1, a, off, 1);
}
DI float max3(float a, float b, float c) { float r; asm("v_max3_f32 %0, %1, %2, %3" : "=v"(r) : "v"(a), "v"(b), "v"(c)); return r; }
DI float max2(float a, float b) { float r; asm("v_max_f32 %0, %1, %2" : "=v"(r) : "v"(a), "v"(b)); return r; }

DI void tail(Ctx& c, LAS char* sm, int wv, f32x4 (&O)[5][3]) {
  const int lane = lane_id();
  if (wv == c.rank) {
#pragma unroll
    for (int n = 0; n < 3; ++n)
#pragma unroll
      for (int ct = 0; ct < 4; ++ct) *(LAS f32x4*)(sm + OWN + (n * 4 + ct) * 1024 + lane * 16) = O[ct][n];
    LBARRIER();
    return;
  }
  const rsrc_t rm = mk(c.ws + WS_M, 1024u * 6144u);
#pragma unroll
  for (int n = 0; n < 3; ++n)
#pragma unroll
    for (int c2 = 0; c2 < 2; ++c2) __builtin_amdgcn_raw_buffer_store_b128(cat(pk4(O[2 * c2][n]), pk4(O[2 * c2 + 1][n])), rm, lane * 16 + (n * 2 + c2) * 1024, (blockIdx.x * 4 + wv) * 6144u, 16);
  const int me = c.rank * 4 + c.j;
  const u64 tok = *(LAS u64*)(sm + MISC + me * 8);
  BARRIER();
  WAITV(0);
  STAMP(28);
  u32 old;
  asm volatile("s_mov_b64 exec, 1\n\tds_add_rtn_u32 %0, %1, %2\n\ts_waitcnt lgkmcnt(0)\n\ts_mov_b64 exec, -1" : "=&v"(old) : "v"((u32)(uintptr_t)sm + MISC + 128), "v"(1u) : "memory");
  if (__builtin_amdgcn_readfirstlane(old) == 2u) st_one(c.ws + WS_F3 + (c.br * 8 + c.bp) * 2048, me * 128, tok, 1ull);
}

DI void compute(Ctx& c, LAS char* sm, int wv) {
  char* ws = c.ws;
  const int bl = wv >> 1, hl = wv & 1;
  const u32 cu = blockIdx.x;
  int lane = lane_id(), g = lane >> 4, m = lane & 15, q = m >> 2, p = lane & 3;
  u32 wbase = (8 * g + q) * 256 + 32 * (q | ((g & 1) << 2)) + 8 * p;
  u32 xbase = (bl * 48 + m) * 128 + ((g ^ (m >> 1)) * 16);
  u32x4 Kf[3][4];
  f32x4 Q[8][3];
  u32x4 A1[8], B1[3];
  LAS char* aw[8]; LAS char* ax[2];
#pragma unroll
  for (int T = 0; T < 8; ++T) { A1[T] = (u32x4){0, 0, 0, 0}; PIN(A1[T]);
    aw[T] = vadd(sm + (wbase ^ (T << 5)), 12288 + hl * 16384);
#pragma unroll
    for (int n = 0; n < 3; ++n) { Q[T][n] = (f32x4){0.f, 0.f, 0.f, 0.f}; PIN(Q[T][n]); } }
#pragma unroll
  for (int n = 0; n < 3; ++n) { B1[n] = (u32x4){0, 0, 0, 0}; PIN(B1[n]); }
  ax[0] = vadd(sm + xbase, 0); ax[1] = vadd(sm + (xbase ^ 64), 0);
  SB();
  STAMP(33); WAITV(0); STAMP(34); BARRIER();
  STAMP(0);
#pragma unroll
  for (int kt = 0; kt < 3; ++kt) {
#pragma unroll 1
    for (int s = 0; s < 3; ++s) {
      u32x4 A0[8], B0[3];
      half1<false, 0, 20>(Q, A1, B1, A0, B0, aw, ax, 0, 0);
      if (s == c.kpos) {
        const u32 l = lane_id(), kb = KS + wv * 4096 + (l & 15) * 256 + ((l >> 5) ^ (l & 15)) * 16 + ((l >> 1) & 8);
#pragma unroll
        for (int i = 0; i < 4; ++i) Kf[kt][i] = cat(lds64(sm + (kb ^ (i << 6))), lds64(sm + (kb ^ (i << 6) ^ 32)));
      }
      half1<false, 20, 24>(Q, A1, B1, A0, B0, aw, ax, 0, 0);
      half1<true, 0, 24>(Q, A0, B0, A1, B1, aw, ax, 1, s == 2 ? -2 * QP : QP);
      LBARRIER();
      STAMP(1 + 3 * kt + s);
    }
  }
  SB();
  lane = lane_id(); g = lane >> 4; m = lane & 15; q = m >> 2; p = lane & 3;
  wbase = (8 * g + q) * 256 + 32 * (q | ((g & 1) << 2)) + 8 * p;
  xbase = (bl * 48 + m) * 128 + ((g ^ (m >> 1)) * 16);
  A2 a;
#pragma unroll
  for (int ct = 0; ct < 4; ++ct) a.w[ct] = sm + (wbase ^ ((4 * hl + ct) << 5));
  a.e = sm + (8 * g + q) * 64 + 32 * (hl ^ (g & 1)) + 8 * p;
  a.v[0] = sm + xbase; a.v[1] = sm + (xbase ^ 64);
  G2 f0, f1;
  f32x4 O[5][3], U[3][5];
  {
    u32x4 qf[3][4];
    f32x4 S[3][3];
#pragma unroll
    for (int i = 0; i < 24; ++i) {
      Q[i & 7][i >> 3] = mfma(A1[i & 7], B1[i >> 3], Q[i & 7][i >> 3]);
      if (i >= 8) { const u32x2 t = pk4(Q[i & 7][(i >> 3) - 1]); qf[(i >> 3) - 1][(i & 7) >> 1][2 * (i & 1)] = t[0]; qf[(i >> 3) - 1][(i & 7) >> 1][2 * (i & 1) + 1] = t[1]; }
      SB();
    }
    STAMP(36);
#pragma unroll
    for (int i = 0; i < 36; ++i) {
      const int n = i / 12, s = (i % 12) / 3, kt = i % 3;
      S[n][kt] = mfma(Kf[kt][s], qf[n][s], s == 0 ? (f32x4){0.f, 0.f, 0.f, 0.f} : S[n][kt]);
      if (i >= 2 && i < 10) { const u32x2 t = pk4(Q[i - 2][2]); qf[2][(i - 2) >> 1][2 * (i & 1)] = t[0]; qf[2][(i - 2) >> 1][2 * (i & 1) + 1] = t[1]; }
      if (i >= 10 && i < 23) rd2<true>(i - 10, f0, a, 0, 0);
      if (i >= 23) rd2<true>(i - 23, f1, a, 0, 1);
      SB();
    }
    STAMP(37);
    const rsrc_t rp = mk(ws + WS_P, 256u * 18432u);
    const u32 pso = cu * 18432u + wv * 4096;
    LAS char* pl = vadd(sm + lane * 16, P_A + wv * 4096);
    LAS char* pl2 = vadd(sm + lane * 8, P_A + wv * 4096 + 3072);
    const int c2o = 16384 - 3072 - wv * 3584;
    float mx[3], sum[3];
    int um = 0;
#define UM() do { if (um < 15) { U[um % 3][um / 3] = mfma(f0.v[um % 3], f0.w[um / 3], (f32x4){0.f, 0.f, 0.f, 0.f}); ++um; } SB(); } while (0)
#pragma unroll
    for (int n = 0; n < 3; ++n)
      mx[n] = max3(max3(S[n][0][0], S[n][0][1], S[n][0][2]), max3(S[n][0][3], S[n][1][0], S[n][1][1]),
                   max3(max3(S[n][1][2], S[n][1][3], S[n][2][0]), max3(S[n][2][1], S[n][2][2], S[n][2][3]), S[n][0][0]));
#pragma unroll
    for (int n = 0; n < 3; ++n) { auto r = __builtin_amdgcn_permlane16_swap(__float_as_uint(mx[n]), __float_as_uint(mx[n]), false, false); mx[n] = max2(__uint_as_float(r[0]), __uint_as_float(r[1])); }
#pragma unroll
    for (int n = 0; n < 3; ++n) { auto r = __builtin_amdgcn_permlane32_swap(__float_as_uint(mx[n]), __float_as_uint(mx[n]), false, false); mx[n] = max2(__uint_as_float(r[0]), __uint_as_float(r[1])) * -0.12751743344863963f; }
    SB();
#pragma unroll
    for (int n = 0; n < 3; ++n)
#pragma unroll
      for (int kt = 0; kt < 3; ++kt) {
#pragma unroll
        for (int i = 0; i < 4; ++i) S[n][kt][i] = __builtin_amdgcn_exp2f(__builtin_fmaf(S[n][kt][i], 0.12751743344863963f, mx[n]));
        UM();
      }
    STAMP(38);
#pragma unroll
    for (int n = 0; n < 3; ++n) { const f32x4 s4 = S[n][0] + S[n][1] + S[n][2]; sum[n] = (s4[0] + s4[1]) + (s4[2] + s4[3]); UM(); }
#pragma unroll
    for (int n = 0; n < 3; ++n) { auto r = __builtin_amdgcn_permlane16_swap(__float_as_uint(sum[n]), __float_as_uint(sum[n]), false, false); sum[n] = __uint_as_float(r[0]) + __uint_as_float(r[1]); }
#pragma unroll
    for (int n = 0; n < 3; ++n) { auto r = __builtin_amdgcn_permlane32_swap(__float_as_uint(sum[n]), __float_as_uint(sum[n]), false, false); sum[n] = __builtin_amdgcn_rcpf(__uint_as_float(r[0]) + __uint_as_float(r[1])); }
    SB();
    STAMP(39);
#pragma unroll
    for (int n = 0; n < 3; ++n) {
      const u32x4 pa = cat(pk4(S[n][0] * sum[n]), pk4(S[n][1] * sum[n]));
      const u32x2 pc = pk4(S[n][2] * sum[n]);
      __builtin_amdgcn_raw_buffer_store_b128(pa, rp, lane * 16 + n * 1024, pso, 16);
      __builtin_amdgcn_raw_buffer_store_b64(pc, rp, lane * 8 + 3072 + (n < 2 ? n * 512 : 0), pso + (n < 2 ? 0 : c2o), 16);
      *(LAS u32x4*)(pl + n * 1024) = pa;
      *(LAS u32x2*)(n < 2 ? pl2 + n * 512 : vadd(pl2, c2o)) = pc;
      UM();
    }
  }
  STAMP(26);
  SB();
  u32x4 UA[5]; u32x2 UC[5];
  const bool xown = (wv == c.rank) && ((c.j >> 1) == (c.rank & 1));
#define ENDI(gi) do { if (((gi) - 9) % 4 == 1) shift(a, 2 * RP); if (((gi) - 9) % 4 == 3) shift(a, -2 * RP); if ((gi) == 19 || (gi) + 1 == XB) WAITV(0); if ((gi) < 22) LBARRIER(); STAMP(1 + (gi)); SB(); } while (0)
#define SLOT(gi) ((((gi) - 9) & 1) * RP)
#define FIN(PE, CE, READ, FIRST, INB, off) do { const int l_ = lane_id(), pb_ = (INB) ? 28672 + bl * 2 * RP : P_A + bl * 8192; \
    finish2<PE, CE, READ, FIRST, (INB) ? RP : 4096>(O, U, UA, UC, f0, f1, a, off, vadd(sm + l_ * 16, pb_), vadd(sm + l_ * 8, pb_ + 3072), \
                                                   vadd(sm + l_ * 8, ((INB) ? P_B2 : P_A + 16384) + bl * 1024)); } while (0)
#define BODY(t, PE, CE) do { \
    constexpr int g0 = 9 + 4 * (t); \
    if ((t) > 0) { \
      FIN(PE, CE, true, (t) == 1, (t) == 3, SLOT(g0)); \
      if ((t) == 2) { \
        if (xown) { _Pragma("unroll") for (int n = 0; n < 3; ++n) *(LAS f32x4*)(sm + XL + n * 1024 + lane_id() * 16) = O[4][n]; } \
        else { const rsrc_t rxx = mk(ws + WS_X, 1024u * 2048u); \
          _Pragma("unroll") for (int n = 0; n < 3; ++n) __builtin_amdgcn_raw_buffer_store_b64(pk4(O[4][n]), rxx, lane_id() * 8 + n * 512, (cu * 4 + wv) * 2048u, 16); } } \
      step2<CE, true, false, false, 0>(U, f0, f1, a, 0, 0, UA, UC); } \
    ENDI(g0); \
    step2<CE, false, true, false, 0>(U, f1, f0, a, SLOT(g0 + 1), 0, UA, UC); \
    step2<CE, false, true, false, 3>(U, f0, f1, a, SLOT(g0 + 1), 1, UA, UC); ENDI(g0 + 1); \
    step2<CE, false, true, false, 0>(U, f1, f0, a, SLOT(g0 + 2), 0, UA, UC); \
    step2<CE, true, true, true, 3>(U, f0, f1, a, SLOT(g0 + 2), 1, UA, UC); ENDI(g0 + 2); \
    step2<CE, false, true, false, 0>(U, f1, f0, a, SLOT(g0 + 3), 0, UA, UC); \
    step2<CE, false, true, false, 3>(U, f0, f1, a, SLOT(g0 + 3), 1, UA, UC); ENDI(g0 + 3); \
  } while (0)
  BODY(0, true, true);
  BODY(1, true, true);
  BODY(2, true, false);
  BODY(3, false, false);
  FIN(false, false, false, false, false, 0);
  STAMP(27);
  tail(c, sm, wv, O);
}

__global__ __launch_bounds__(512, 2) void fused_attn_block(const char* xA, const char* kA, const char* vA, const char* wqA, const char* woA,
                                                          const char* xB, const char* kB, const char* vB, const char* wqB, const char* woB,
                                                          char* out, char* ws) {
  __shared__ __attribute__((aligned(256))) char smem[LDS_BYTES];
  LAS char* sm = (LAS char*)smem;
  const int wv = __builtin_amdgcn_readfirstlane(threadIdx.x >> 6);
  Ctx c;
  c.stamps = 0; c.ta = 0; c.tb = 0;
  STAMP(63);
  const int id = blockIdx.x;
  c.br = id & 1; c.rank = (id >> 1) & 3; c.j = (id >> 3) & 3; c.bp = id >> 5;
  c.h0 = c.rank * 8; c.kvsh = 1 - c.br;
  c.hp = ROTH ? (c.j + c.bp) & 3 : c.j;
  { const int g3 = (c.bp + c.hp) % 3;
    c.ord = g3 == 0 ? 0x875462130ull : g3 == 1 ? 0x865732401ull : 0x768435102ull;
    c.kpos = KPOS ? KPOS : (g3 == 1 ? 2 : 1); }
  c.rot = ROTK == 4 ? (int)(c.ord & 15) : ROTK == 1 ? (c.bp + c.hp) % 3 : ROTK == 3 ? ((c.bp + c.hp) % 3) * 3 : ROTK == 2 ? ((c.bp + c.hp) & 1) * 4 : 0;
  c.x = c.br ? xB : xA; c.k = c.br ? kB : kA; c.v = c.br ? vB : vA; c.wq = c.br ? wqB : wqA; c.wo = c.br ? woB : woA;
  c.out = out; c.ws = ws;
  asm volatile("s_getpc_b64 %0" : "=s"(c.pc));
  start(c, sm, lane_id(), wv);
  u64 tok = 0;
  { int w2 = wv; asm volatile("" : "+s"(w2));
    if (__builtin_expect(w2 >= 4, 1)) tok = helper1(c, sm, w2 - 4); }
  if (__builtin_expect(wv < 4, 1)) {
    if (ABL_NOCOMP) { STAMP(33); WAITV(0); STAMP(34); BARRIER(); STAMP(0); for (int g = 1; g < 24; ++g) { BARRIER(); STAMP(g < 23 ? g : g + 2); } }
    else compute(c, sm, wv);
  } else helper2(c, sm, wv - 4, tok);
}

extern "C" void kernel_launch(void* const* d_in, const int* in_sizes, int n_in, void* d_out, int out_size, void* d_ws, size_t ws_size,
                              hipStream_t stream) {
  const char* const* in = (const char* const*)d_in;
  fused_attn_block<<<256, 512, 0, stream>>>(in[0], in[1], in[2], in[3], in[4], in[5], in[6], in[7], in[8], in[9], (char*)d_out, (char*)d_ws);
}
```

```cpp
#include <hip/hip_runtime.h>
#include <stdint.h>

typedef unsigned u32;
typedef unsigned long long u64;
typedef u32 u32x4 __attribute__((ext_vector_type(4)));
typedef u32 u32x2 __attribute__((ext_vector_type(2)));
typedef float f32x4 __attribute__((ext_vector_type(4)));
typedef float f32x2 __attribute__((ext_vector_type(2)));
typedef short s16x4 __attribute__((ext_vector_type(4)));
typedef __bf16 bf16x8 __attribute__((ext_vector_type(8)));
typedef __bf16 bf16x2 __attribute__((ext_vector_type(2)));
#define LAS __attribute__((address_space(3)))
#define GAS __attribute__((address_space(1)))
#define DI __device__ __forceinline__
typedef __amdgpu_buffer_rsrc_t rsrc_t;

constexpr int QP = 45056;
constexpr int RP = 32768;
constexpr int KS = 135168;
constexpr int P_A = 131072;
constexpr int XL = 149504;
constexpr int P_B2 = 154880;
constexpr int OWN = 0;
constexpr int MISC = 153600;
constexpr int JUNK = 153856;
constexpr int LDS_BYTES = 156928;
constexpr u32 WS_F1 = 0;
constexpr u32 WS_F3 = 32768;
constexpr u32 WS_R = 65536;
constexpr u32 WS_P = 131072;
constexpr u32 WS_M = WS_P + 256u * 18432u;
constexpr u32 WS_X = WS_M + 1024u * 6144u;
constexpr u64 TOKK = 0x9E3779B97F4A7C15ull;
constexpr u32 SPIN_CAP = 1u << 20;
constexpr u32 CODE_KB = 30;

DI rsrc_t mk(const void* p, u32 bytes) { return __builtin_amdgcn_make_buffer_rsrc((void*)p, 0, bytes, 0x00020000); }
DI void dma_asm(rsrc_t r, u32 dst, u32 voff, u32 soff) {
  asm volatile("s_mov_b32 m0, %0\n\ts_nop 0\n\tbuffer_load_dwordx4 %1, %2, %3 offen lds" :: "s"(dst), "v"(voff), "s"(r), "s"(soff) : "memory", "m0");
}
template <int N, int STEP> DI void dma_run(rsrc_t r, u32 dst, u32 voff, u32 soff) {
  u32 t;
#define PIECE_ "s_add_u32 m0, m0, 0x1000\n\ts_add_u32 %0, %0, %5\n\tbuffer_load_dwordx4 %2, %3, %0 offen lds\n\t"
#define FIRST_ "s_mov_b32 m0, %1\n\ts_mov_b32 %0, %4\n\tbuffer_load_dwordx4 %2, %3, %0 offen lds\n\t"
  if (N == 3) asm volatile(FIRST_ PIECE_ PIECE_ : "=&s"(t) : "s"(dst), "v"(voff), "s"(r), "s"(soff), "n"(STEP) : "memory", "m0", "scc");
  if (N == 4) asm volatile(FIRST_ PIECE_ PIECE_ PIECE_ : "=&s"(t) : "s"(dst), "v"(voff), "s"(r), "s"(soff), "n"(STEP) : "memory", "m0", "scc");
}
#define WAITV(n) asm volatile("s_waitcnt vmcnt(%0)" ::"i"(n) : "memory")
#define BARRIER() asm volatile("s_barrier" ::: "memory")
#define LBARRIER() do { __builtin_amdgcn_s_waitcnt(0xC07F); asm volatile("s_barrier" ::: "memory"); } while (0)
#define SB() __builtin_amdgcn_sched_barrier(0)
#define PIN(x) asm volatile("" : "+v"(x))
DI int lane_id() { int l = __builtin_amdgcn_mbcnt_hi(~0u, __builtin_amdgcn_mbcnt_lo(~0u, 0u)); PIN(l); return l; }
DI f32x4 mfma(u32x4 a, u32x4 b, f32x4 c) { return __builtin_amdgcn_mfma_f32_16x16x32_bf16(__builtin_bit_cast(bf16x8, a), __builtin_bit_cast(bf16x8, b), c, 0, 0, 0); }
DI u32 pk(float a, float b) { f32x2 f = {a, b}; return __builtin_bit_cast(u32, __builtin_convertvector(f, bf16x2)); }
DI u32x2 pk4(f32x4 v) { u32x2 r = {pk(v[0], v[1]), pk(v[2], v[3])}; return r; }
DI u32x4 cat(u32x2 a, u32x2 b) { u32x4 r = {a[0], a[1], b[0], b[1]}; return r; }
DI u32x2 trd(LAS char* p) { return __builtin_bit_cast(u32x2, __builtin_amdgcn_ds_read_tr16_b64_v4i16((LAS s16x4*)p)); }
DI u32x4 lds128(LAS char* p) { return *(LAS u32x4*)p; }
DI u32x2 lds64(LAS char* p) { return *(LAS u32x2*)p; }
DI float bflo(u32 x) { return __builtin_bit_cast(float, x << 16); }
DI float bfhi(u32 x) { return __builtin_bit_cast(float, x & 0xffff0000u); }
DI u64 rdlane64(u64 v, int l) { return ((u64)(u32)__builtin_amdgcn_readlane((int)(v >> 32), l) << 32) | (u32)__builtin_amdgcn_readlane((int)v, l); }
DI LAS char* vadd(LAS char* p, int d) { u32 r; asm volatile("v_add_u32 %0, %1, %2" : "=v"(r) : "s"(d), "v"((u32)(uintptr_t)p)); return (LAS char*)(uintptr_t)r; }

struct Ctx {
  const char *x, *k, *v, *wq, *wo;
  u64 pc;
  char *out, *ws;
  u64 ord;
  int br, rank, bp, j;
  int h0, kvsh, hp, kpos;
};


DI void start(Ctx& c, LAS char* sm, int lane, int wv) {
  const rsrc_t rx = mk(c.x, 768 * 1152), rq = mk(c.wq, 576 * 8192);
  const u32 l4 = lane >> 4;
  const u32 vx = (lane >> 3) * 1152 + ((lane & 7) ^ (l4 | ((wv & 1) << 2))) * 16;
  const u32 vq = l4 * 8192 + ((lane & 15) ^ ((l4 | ((wv & 2) << 1)) << 1)) * 16;
  const u32 k0 = (u32)c.ord & 15u;
  const u32 xs = (u32)(c.bp * 96 + wv * 8) * 1152u + k0 * 128, qs = (u32)(c.h0 + 2 * c.hp) * 256u + wv * (4 * 8192) + k0 * (64 * 8192);
  const u32 d = (u32)(uintptr_t)sm + wv * 1024;
  dma_asm(rx, d, vx, xs);
#pragma unroll
  for (int i = 0; i < 4; ++i) dma_asm(rq, d + 12288 + i * 8192, vq, qs + (i & 1) * (32 * 8192) + (i >> 1) * 256);
  dma_asm(rx, wv < 4 ? d + 8192 : (u32)(uintptr_t)sm + JUNK, vx, wv < 4 ? xs + 64 * 1152 : 0x7f000000u);

}

DI u64 ld_async(const char* base, u32 off) { u64 r; asm volatile("global_load_dwordx2 %0, %1, %2 sc1" : "=v"(r) : "v"(off), "s"(base) : "memory"); return r; }
DI u64 ld_now(const char* base, u32 off) { u64 r; asm volatile("global_load_dwordx2 %0, %1, %2 sc1\n\ts_waitcnt vmcnt(0)" : "=&v"(r) : "v"(off), "s"(base) : "memory"); return r; }
DI void st_one(const char* base, u32 off, u64 v, u64 lanes) {
  asm volatile("s_mov_b64 exec, %3\n\tglobal_store_dwordx2 %0, %1, %2 sc1\n\ts_mov_b64 exec, -1" :: "v"(off), "v"(v), "s"(base), "s"(lanes) : "memory");
}

DI u64 helper1(Ctx& c, LAS char* sm, int l) {
  const int lane = lane_id(), hkv = 32 >> c.kvsh;
  const u32 base = (u32)(uintptr_t)sm;
  const rsrc_t rx = mk(c.x, 768 * 1152), rq = mk(c.wq, 576 * 8192), rk = mk(c.k, (u32)(16 * hkv) * (48 * 256));
  const u32 l4 = lane >> 4;
  const u32 vx = (lane >> 3) * 1152 + ((lane & 7) ^ (l4 | ((l & 1) << 2))) * 16;
  const u32 vq = l4 * 8192 + ((lane & 15) ^ ((l4 | ((l & 2) << 1)) << 1)) * 16;
  const u32 vk = l4 * 256 + ((lane & 15) ^ (l4 | (l << 2))) * 16;
  const u32 xs = (u32)(c.bp * 96 + l * 8) * 1152u, qs = (u32)(c.h0 + 2 * c.hp) * 256u + l * (4 * 8192);
  const u32 k0 = (u32)(2 * c.bp * hkv + ((c.h0 + 2 * c.hp) >> c.kvsh)) * (48 * 256) + l * 1024, k1 = c.kvsh ? 0u : 48u * 256u, k2 = (u32)hkv * (48 * 256);
  const int sg = c.br * 8 + c.bp, me = c.rank * 4 + c.j, m = lane & 15;
  u64 tok = 0;

#define STAGE(st_, slot_) do { const u32 k_ = (u32)(c.ord >> (4 * (st_))) & 15u, d_ = base + (slot_) * QP + l * 1024; \
    dma_run<3, 32 * 1152>(rx, d_, vx, xs + k_ * 128); \
    dma_run<4, 16 * 8192>(rq, d_ + 12288, vq, qs + k_ * (64 * 8192)); \
    dma_run<4, 16 * 8192>(rq, d_ + 28672, vq, qs + k_ * (64 * 8192) + 256); } while (0)
#define KTILE(kt_) do { _Pragma("unroll") for (int i = 0; i < 4; ++i) dma_asm(rk, base + KS + l * 1024 + i * 4096, vk, k0 + (i & 1) * k1 + (i >> 1) * k2 + (kt_) * 4096); } while (0)
  STAGE(1, 1);
  if (c.kpos == 1) { KTILE(0); WAITV(15); } else WAITV(11);
 BARRIER();
  { u32 line = ((((blockIdx.x >> 3) & 1) * 4 + l) * 64 + lane); line = line < CODE_KB * 8 ? line : 0u;
    asm volatile("s_mov_b32 m0, %0\n\ts_nop 0\n\tglobal_load_lds_dword %1, %2" :: "s"(base + JUNK), "v"(line * 128), "s"(c.pc & ~255ull) : "memory", "m0"); }
  tok = ld_async(c.ws + WS_F1 + sg * 2048, m * 128);
  STAGE(2, 2);
  if (c.kpos == 2) { KTILE(0); WAITV(15); } else WAITV(11);
  PIN(tok); BARRIER();
  tok += TOKK;
  if (l == 3) {
    st_one(c.ws + WS_R + sg * 2048, m * 128 + me * 8, tok, 0xffffull);
    *(LAS u64*)(sm + MISC + m * 8) = tok; *(LAS u32*)(sm + MISC + 128) = 0u;
    asm volatile("s_waitcnt lgkmcnt(0)" ::: "memory");
  }
  int slot = 0, kt = 1;
#pragma unroll 1
  for (int st = 3; st < 9; ++st) {
    STAGE(st, slot);
    if (slot == c.kpos) { KTILE(kt); ++kt; WAITV(15); } else WAITV(11);
    slot = slot == 2 ? 0 : slot + 1;
    BARRIER();
  }
  return tok;
}

struct H2 {
  rsrc_t ro, rv;
  u32 vo, vv, ve, oso, eso, vs[3], base;
  int hown, h0, kvsh;
  template <bool EXT> DI void p2(int st) {
    const int h = h0 + ((hown + (st >> 1)) & 7);
    const u32 row = (u32)(h * 128 + (st & 1) * 64) * 1152u, vh = (u32)(h >> kvsh) * (48 * 256) + (st & 1) * 128, d = base + (st & 3) * RP;
#pragma unroll
    for (int i = 0; i < 3; ++i) dma_asm(rv, d + 16384 + i * 4096, vv, vs[i] + vh);
    dma_run<4, 16 * 1152>(ro, d, vo, oso + row);
    if (EXT) dma_asm(ro, d + 28672, ve, eso + row);
  }
};

DI void helper2(Ctx& c, LAS char* sm, int l, u64 tok) {
  const int lane = lane_id(), hkv = 32 >> c.kvsh, l4 = lane >> 4;
  H2 z;
  z.ro = mk(c.wo, 4096 * 1152); z.rv = mk(c.v, (u32)(16 * hkv) * (48 * 256));
  z.vo = l4 * 1152 + ((lane & 15) ^ ((l4 | ((l & 2) << 1)) << 1)) * 16;
  z.vv = (lane >> 3) * 256 + ((lane & 7) ^ (l4 | ((l & 1) << 2))) * 16;
  z.ve = (lane >> 2) * 1152 + ((lane & 3) ^ ((lane >> 5) << 1)) * 16;
  z.oso = c.j * 256 + l * (4 * 1152); z.eso = 1024 + (c.j & 1) * 64 + l * (16 * 1152);
  { const u32 b0 = (u32)(2 * c.bp * hkv) * (48 * 256), b1 = b0 + (u32)hkv * (48 * 256);
    z.vs[0] = b0 + l * 2048; z.vs[1] = l < 2 ? b0 + (l + 4) * 2048 : b1 + (l - 2) * 2048; z.vs[2] = b1 + (l + 2) * 2048; }
  z.base = (u32)(uintptr_t)sm + l * 1024; z.hown = 2 * c.hp; z.h0 = c.h0; z.kvsh = c.kvsh;
  const int sg = c.br * 8 + c.bp, me = c.rank * 4 + c.j;
  const char* F1 = c.ws + WS_F1 + sg * 2048;
  z.p2<true>(0); WAITV(8); BARRIER();
  z.p2<true>(1); WAITV(8); BARRIER();
  const u64 mytok = rdlane64(tok, me);
  const rsrc_t rp = mk(c.ws + WS_P, 256u * 18432u);
  const u32 p4 = 16384 + (l & 1) * 1024;
  LAS char* pl = sm + P_A + l * 1024 + lane * 16;
  LAS char* pl4 = sm + P_A + p4 + lane * 16;
  LAS char* pm = sm + 28672 + l * 1024 + lane * 16;
  LAS char* pm4 = sm + P_B2 + (l & 1) * 1024 + lane * 16;
  const int g = lane >> 4, m = lane & 15, hl = c.rank & 1;
  const bool xown = (c.j >> 1) == hl;
  const u32 cu = blockIdx.x, orow = (u32)((2 * c.bp + (c.rank >> 1)) * 48 + m) * 2304u + c.br * 1152;
  const bool need = m != me && (l < 3 ? (m & 3) == c.j : ((m ^ c.j) & 1) == 0);
  {
    u64 r = ld_async(c.ws + WS_R + sg * 2048, me * 128 + m * 8);
    z.p2<true>(2); z.p2<true>(3); WAITV(16); PIN(r);
    if (!__all(r == mytok))
      for (u32 spins = 0; spins < SPIN_CAP; ++spins) if (__all(ld_now(c.ws + WS_R + sg * 2048, me * 128 + m * 8) == mytok)) break;
  }
  BARRIER();
  z.p2<true>(4); WAITV(16); BARRIER();
  if (l == 3) st_one(F1, me * 128, mytok, 1ull);
  z.p2<true>(5); WAITV(16); BARRIER();

  z.p2<true>(6); WAITV(16); BARRIER();
#define PLOAD(pv, t, i) asm volatile("buffer_load_dwordx4 %0, %1, %2, %3 offen sc1" : "=v"(pv[i]) : "v"(lane * 16), "s"(rp), \
    "s"((u32)((c.bp * 4 + ((c.j + (t)) & 3)) * 8 + c.rank * 2 + c.br) * 18432u + ((i) < 4 ? l * 1024 + (i) * 4096 : p4)) : "memory")
#define PUTA(pv, i) do { PIN(pv[i]); if ((i) < 4 || l < 2) *(LAS u32x4*)((i) < 4 ? pl + (i) * 4096 : pl4) = pv[i]; } while (0)
#define PUTB(pv, i) do { PIN(pv[i]); if ((i) < 4 || l < 2) *(LAS u32x4*)((i) < 2 ? pm + (i) * RP : (i) < 4 ? pm + 2 * RP + ((i) - 2) * RP : pm4) = pv[i]; } while (0)
  u32x4 pa[5], pb[5], pc[5];
  {
    const u64 want = __shfl(tok, c.rank * 4 + (lane & 3));
    u64 r = ld_async(F1, (c.rank * 4 + (lane & 3)) * 128);
    z.p2<true>(7); WAITV(8); PIN(r);
    if (!__all(r == want))
      for (u32 spins = 0; spins < SPIN_CAP; ++spins) if (__all(ld_now(F1, (c.rank * 4 + (lane & 3)) * 128) == want)) break;
  }

  PLOAD(pa, 1, 0); PLOAD(pa, 1, 1); PLOAD(pa, 1, 2); PLOAD(pa, 1, 3); PLOAD(pa, 1, 4); WAITV(13); BARRIER();
  z.p2<false>(8); WAITV(7); PUTA(pa, 0); BARRIER();
  PUTA(pa, 1); PUTA(pa, 2); z.p2<false>(9); PLOAD(pb, 2, 0); PLOAD(pb, 2, 1); BARRIER();
  PUTA(pa, 3); PUTA(pa, 4); z.p2<false>(10); PLOAD(pb, 2, 2); WAITV(17); LBARRIER();
  PLOAD(pb, 2, 3); PLOAD(pb, 2, 4); z.p2<false>(11); WAITV(7);
  PUTB(pb, 0); PUTB(pb, 1); PUTB(pb, 2); PUTB(pb, 3); PUTB(pb, 4); BARRIER();
  z.p2<false>(12); PLOAD(pc, 3, 0); PLOAD(pc, 3, 1); BARRIER();
  z.p2<false>(13); PLOAD(pc, 3, 2); WAITV(17); BARRIER();
  if (l == 3) st_one(c.ws + WS_F3 + sg * 2048, me * 128 + 8, mytok, 1ull);
  z.p2<false>(14); PLOAD(pc, 3, 3); PLOAD(pc, 3, 4); WAITV(19); LBARRIER();
  z.p2<false>(15); WAITV(7);
  PUTA(pc, 0); PUTA(pc, 1); PUTA(pc, 2); PUTA(pc, 3); PUTA(pc, 4); WAITV(0); LBARRIER();
  BARRIER();
  if (l == 3 && !xown) return;
  for (u32 spins = 0; spins < SPIN_CAP; ++spins) if (__all(!need || ld_now(c.ws + WS_F3 + sg * 2048, m * 128 + (l == 3 ? 8 : 0)) == tok)) break;
  {
    const rsrc_t ro = mk(c.out, 768 * 2304);
    if (l < 3) {
      const rsrc_t rm = mk(c.ws + WS_M, 1024u * 6144u);
      u32x4 in[3][2];
#pragma unroll
      for (int r = 0; r < 3; ++r) {
        const u32 pcu = (cu & ~6u) | (((c.rank + 1 + r) & 3) << 1);
#pragma unroll
        for (int i = 0; i < 2; ++i) in[r][i] = __builtin_amdgcn_raw_buffer_load_b128(rm, lane * 16 + i * 1024, (pcu * 4 + c.rank) * 6144u + l * 2048, 16);
      }
#pragma unroll
      for (int c2 = 0; c2 < 2; ++c2) {
        f32x4 a = *(LAS f32x4*)(sm + OWN + (l * 4 + 2 * c2) * 1024 + lane * 16), d = *(LAS f32x4*)(sm + OWN + (l * 4 + 2 * c2 + 1) * 1024 + lane * 16);
#pragma unroll
        for (int r = 0; r < 3; ++r) {
          const u32x4 v = in[r][c2];
          a += (f32x4){bflo(v[0]), bfhi(v[0]), bflo(v[1]), bfhi(v[1])};
          d += (f32x4){bflo(v[2]), bfhi(v[2]), bflo(v[3]), bfhi(v[3])};
        }
        const u32x2 pa = pk4(a), pd = pk4(d);
        auto s0 = __builtin_amdgcn_permlane16_swap(pa[0], pd[0], false, false);
        auto s1 = __builtin_amdgcn_permlane16_swap(pa[1], pd[1], false, false);
        __builtin_amdgcn_raw_buffer_store_b128((u32x4){s0[0], s1[0], s0[1], s1[1]}, ro,
            orow + l * (16 * 2304) + (128 * c.j + 64 * hl + 32 * c2 + 16 * (g & 1) + 8 * (g >> 1)) * 2, 0, 0);
      }
    } else {
      const rsrc_t rxx = mk(c.ws + WS_X, 1024u * 2048u);
      u32x2 xin[7][3];
#pragma unroll
      for (int r = 0; r < 7; ++r) {
        const u32 pcu = ((cu & ~6u) | (((c.rank + 1 + r) & 3) << 1)) ^ (r >= 3 ? 16u : 0u);
#pragma unroll
        for (int n = 0; n < 3; ++n) xin[r][n] = __builtin_amdgcn_raw_buffer_load_b64(rxx, lane * 8 + n * 512, (pcu * 4 + c.rank) * 2048u, 16);
      }
#pragma unroll
      for (int n = 0; n < 3; ++n) {
        f32x4 a = *(LAS f32x4*)(sm + XL + n * 1024 + lane * 16);
#pragma unroll
        for (int r = 0; r < 7; ++r) a += (f32x4){bflo(xin[r][n][0]), bfhi(xin[r][n][0]), bflo(xin[r][n][1]), bfhi(xin[r][n][1])};
        __builtin_amdgcn_raw_buffer_store_b64(pk4(a), ro, orow + n * (16 * 2304) + (512 + 32 * (c.j & 1) + 16 * hl + 4 * g) * 2, 0, 0);
      }
    }
  }

}

template <bool ADV, int I0, int I1> DI void half1(f32x4 (&Q)[8][3], const u32x4 (&A)[8], const u32x4 (&B)[3], u32x4 (&An)[8], u32x4 (&Bn)[3],
                                  LAS char* (&w)[8], LAS char* (&x)[2], int u, int adv) {
  u32x2 lo[8];
#pragma unroll
  for (int i = I0; i < I1; ++i) {
    Q[i / 3][i % 3] = mfma(A[i / 3], B[i % 3], Q[i / 3][i % 3]);
    constexpr int what[19] = {70, 71, 00, 01, 100, 10, 11, 101, 20, 21, 102, 30, 31, 60, 40, 41, 61, 50, 51};
    if (i < 19) {
      const int k = what[i];
      if (k >= 100) Bn[k - 100] = lds128(x[u] + (k - 100) * 2048);
      else if (k % 10 == 0) lo[k / 10] = trd(w[k / 10] + u * 8192);
      else An[k / 10] = cat(lo[k / 10], trd(w[k / 10] + u * 8192 + 1024));
    } else if (ADV) {
      if (i < 23) { w[2 * (i - 19)] = vadd(w[2 * (i - 19)], adv); w[2 * (i - 19) + 1] = vadd(w[2 * (i - 19) + 1], adv); }
      else { x[0] = vadd(x[0], adv); x[1] = vadd(x[1], adv); }
    }
    SB();
  }
}
struct G2 { u32x4 v[3], w[5]; u32x2 lo; };
struct A2 { LAS char* w[4]; LAS char* e; LAS char* v[2]; };
template <bool EXT> DI void rd2(int i, G2& f, const A2& a, int off, int u) {
  if (i < 3) f.v[i] = lds128(a.v[u] + off + 16384 + i * 2048);
  else if (i < 11) { const int ct = (i - 3) >> 1; if ((i - 3) & 1) f.w[ct] = cat(f.lo, trd(a.w[ct] + off + u * 8192 + 1024)); else f.lo = trd(a.w[ct] + off + u * 8192); }
  else if (EXT && i == 11) f.lo = trd(a.e + off + 28672 + u * 2048);
  else if (EXT && i == 12) f.w[4] = cat(f.lo, trd(a.e + off + 28672 + u * 2048 + 256));
}
template <bool E, bool FIRST, bool READ, bool PACK, int DBL, int SH = 0>
DI void step2(f32x4 (&U)[3][5], const G2& f, G2& fn, A2& a, int off, int u, u32x4 (&UA)[5], u32x2 (&UC)[5]) {
  const f32x4 z = {0.f, 0.f, 0.f, 0.f};
#pragma unroll
  for (int i = 0; i < (E ? 15 : 12); ++i) {
    const int ct = i / 3, kt = i % 3;
    if (PACK) { const u32x2 t = pk4(U[kt][ct]); if (kt == 0) { UA[ct][0] = t[0]; UA[ct][1] = t[1]; } else if (kt == 1) { UA[ct][2] = t[0]; UA[ct][3] = t[1]; } else UC[ct] = t; SB(); }
    U[kt][ct] = mfma(f.v[kt], f.w[ct], FIRST ? z : U[kt][ct]);
    if (READ) { if (i < DBL) { rd2<E>(2 * i, fn, a, off, u); rd2<E>(2 * i + 1, fn, a, off, u); } else rd2<E>(i + DBL, fn, a, off, u); }
    if (SH) { const int k = i - (E ? 11 : 8);
      if (k == 0) { a.v[0] = vadd(a.v[0], SH); a.v[1] = vadd(a.v[1], SH); }
      if (k == 1) { a.w[0] = vadd(a.w[0], SH); a.w[1] = vadd(a.w[1], SH); }
      if (k == 2) { a.w[2] = vadd(a.w[2], SH); a.w[3] = vadd(a.w[3], SH); }
      if (k == 3) a.e = vadd(a.e, SH); }
    SB();
  }
}
template <bool PE, bool CE, bool READ, bool FIRST, int BS>
DI void finish2(f32x4 (&O)[5][3], f32x4 (&U)[3][5], const u32x4 (&A0)[5], const u32x2 (&C0)[5], G2& f0, G2& f1, const A2& a, int off, LAS char* p, LAS char* pc, LAS char* pc2) {
  constexpr int N = PE ? 15 : 12;
  u32x4 pa0[3], pa1[3], pcc[3], A1[5]; u32x2 C1[5], lo[3];
#pragma unroll
  for (int i = 0; i < N; ++i) {
    U[i % 3][i / 3] = mfma(f1.v[i % 3], f1.w[i / 3], U[i % 3][i / 3]);
    if (i < 3) pa0[i] = lds128(p + i * 1024);
    else if (i < 6) pa1[i - 3] = lds128(p + BS + (i - 3) * 1024);
    else if (READ) rd2<CE>(i - 6, f0, a, off, 0);
    SB();
  }
#pragma unroll
  for (int i = 0; i < N; ++i) {
    const int ct = i / 3, n = i % 3;
    O[ct][n] = mfma(A0[ct], pa0[n], FIRST ? (f32x4){0.f, 0.f, 0.f, 0.f} : O[ct][n]);
    const u32x2 t = pk4(U[n][ct]);
    if (n == 0) { A1[ct][0] = t[0]; A1[ct][1] = t[1]; } else if (n == 1) { A1[ct][2] = t[0]; A1[ct][3] = t[1]; } else C1[ct] = t;
    SB();
  }
#pragma unroll
  for (int i = 0; i < N; ++i) {
    O[i / 3][i % 3] = mfma(A1[i / 3], pa1[i % 3], O[i / 3][i % 3]);
    if (i < 3) lo[i] = lds64(i < 2 ? pc + i * 512 : pc2);
    else if (i < 6) pcc[i - 3] = cat(lo[i - 3], lds64(i < 5 ? pc + BS + (i - 3) * 512 : pc2 + 512));
    else if (READ) rd2<CE>(i + N - 12, f0, a, off, 0);
    SB();
  }
  if (READ && N == 12) rd2<CE>(12, f0, a, off, 0);
#pragma unroll
  for (int i = 0; i < N; ++i) {
    O[i / 3][i % 3] = mfma(cat(C0[i / 3], C1[i / 3]), pcc[i % 3], O[i / 3][i % 3]);
    if (READ) rd2<CE>(i, f1, a, off, 1);
    SB();
  }
  if (READ && N == 12) rd2<CE>(12, f1, a, off, 1);
}

DI void tail(Ctx& c, LAS char* sm, int wv, f32x4 (&O)[5][3]) {
  const int lane = lane_id();
  if (wv == c.rank) {
#pragma unroll
    for (int n = 0; n < 3; ++n)
#pragma unroll
      for (int ct = 0; ct < 4; ++ct) *(LAS f32x4*)(sm + OWN + (n * 4 + ct) * 1024 + lane * 16) = O[ct][n];
    LBARRIER();
    return;
  }
  const rsrc_t rm = mk(c.ws + WS_M, 1024u * 6144u);
#pragma unroll
  for (int n = 0; n < 3; ++n)
#pragma unroll
    for (int c2 = 0; c2 < 2; ++c2) __builtin_amdgcn_raw_buffer_store_b128(cat(pk4(O[2 * c2][n]), pk4(O[2 * c2 + 1][n])), rm, (blockIdx.x * 4 + wv) * 6144u + lane * 16 + (n * 2 + c2) * 1024, 0, 16);
  const int me = c.rank * 4 + c.j;
  const u64 tok = *(LAS u64*)(sm + MISC + me * 8);
  BARRIER();
  WAITV(0);

  u32 old;
  asm volatile("s_mov_b64 exec, 1\n\tds_add_rtn_u32 %0, %1, %2\n\ts_waitcnt lgkmcnt(0)\n\ts_mov_b64 exec, -1" : "=&v"(old) : "v"((u32)(uintptr_t)sm + MISC + 128), "v"(1u) : "memory");
  if (__builtin_amdgcn_readfirstlane(old) == 2u) st_one(c.ws + WS_F3 + (c.br * 8 + c.bp) * 2048, me * 128, tok, 1ull);
}

DI void compute(Ctx& c, LAS char* sm, int wv) {
  char* ws = c.ws;
  const int bl = wv >> 1, hl = wv & 1;
  const u32 cu = blockIdx.x;
  int lane = lane_id(), g = lane >> 4, m = lane & 15, q = m >> 2, p = lane & 3;
  u32 wbase = (8 * g + q) * 256 + 32 * (q | ((g & 1) << 2)) + 8 * p;
  u32 xbase = (bl * 48 + m) * 128 + ((g ^ (m >> 1)) * 16);
  u32x4 Kf[3][4];
  f32x4 Q[8][3];
  u32x4 A1[8], B1[3];
  LAS char* aw[8]; LAS char* ax[2];
#pragma unroll
  for (int T = 0; T < 8; ++T) { A1[T] = (u32x4){0, 0, 0, 0}; PIN(A1[T]);
    aw[T] = vadd(sm + (wbase ^ (T << 5)), 12288 + hl * 16384);
#pragma unroll
    for (int n = 0; n < 3; ++n) { Q[T][n] = (f32x4){0.f, 0.f, 0.f, 0.f}; PIN(Q[T][n]); } }
#pragma unroll
  for (int n = 0; n < 3; ++n) { B1[n] = (u32x4){0, 0, 0, 0}; PIN(B1[n]); }
  ax[0] = vadd(sm + xbase, 0); ax[1] = vadd(sm + (xbase ^ 64), 0);
  SB();
 WAITV(0); BARRIER();

#pragma unroll
  for (int kt = 0; kt < 3; ++kt) {
#pragma unroll 1
    for (int s = 0; s < 3; ++s) {
      u32x4 A0[8], B0[3];
      half1<false, 0, 20>(Q, A1, B1, A0, B0, aw, ax, 0, 0);
      if (s == c.kpos) {
        const u32 l = lane_id(), kb = KS + wv * 4096 + (l & 15) * 256 + ((l >> 5) ^ (l & 15)) * 16 + ((l >> 1) & 8);
#pragma unroll
        for (int i = 0; i < 4; ++i) Kf[kt][i] = cat(lds64(sm + (kb ^ (i << 6))), lds64(sm + (kb ^ (i << 6) ^ 32)));
      }
      half1<false, 20, 24>(Q, A1, B1, A0, B0, aw, ax, 0, 0);
      half1<true, 0, 24>(Q, A0, B0, A1, B1, aw, ax, 1, s == 2 ? -2 * QP : QP);
      LBARRIER();

    }
  }
  SB();
  lane = lane_id(); g = lane >> 4; m = lane & 15; q = m >> 2; p = lane & 3;
  wbase = (8 * g + q) * 256 + 32 * (q | ((g & 1) << 2)) + 8 * p;
  xbase = (bl * 48 + m) * 128 + ((g ^ (m >> 1)) * 16);
  A2 a;
#pragma unroll
  for (int ct = 0; ct < 4; ++ct) a.w[ct] = sm + (wbase ^ ((4 * hl + ct) << 5));
  a.e = sm + (8 * g + q) * 64 + 32 * (hl ^ (g & 1)) + 8 * p;
  a.v[0] = sm + xbase; a.v[1] = sm + (xbase ^ 64);
  G2 f0, f1;
  f32x4 O[5][3], U[3][5];
  {
    u32x4 qf[3][4];
    f32x4 S[3][3];
#pragma unroll
    for (int i = 0; i < 24; ++i) {
      Q[i & 7][i >> 3] = mfma(A1[i & 7], B1[i >> 3], Q[i & 7][i >> 3]);
      if (i >= 8) { const u32x2 t = pk4(Q[i & 7][(i >> 3) - 1]); qf[(i >> 3) - 1][(i & 7) >> 1][2 * (i & 1)] = t[0]; qf[(i >> 3) - 1][(i & 7) >> 1][2 * (i & 1) + 1] = t[1]; }
      SB();
    }

#pragma unroll
    for (int i = 0; i < 36; ++i) {
      const int n = i / 12, s = (i % 12) / 3, kt = i % 3;
      S[n][kt] = mfma(Kf[kt][s], qf[n][s], s == 0 ? (f32x4){0.f, 0.f, 0.f, 0.f} : S[n][kt]);
      if (i >= 2 && i < 10) { const u32x2 t = pk4(Q[i - 2][2]); qf[2][(i - 2) >> 1][2 * (i & 1)] = t[0]; qf[2][(i - 2) >> 1][2 * (i & 1) + 1] = t[1]; }
      if (i >= 10 && i < 23) rd2<true>(i - 10, f0, a, 0, 0);
      if (i >= 23) rd2<true>(i - 23, f1, a, 0, 1);
      SB();
    }

    const rsrc_t rp = mk(ws + WS_P, 256u * 18432u);
    const u32 pso = cu * 18432u + wv * 4096;
    LAS char* pl = vadd(sm + lane * 16, P_A + wv * 4096);
    LAS char* pl2 = vadd(sm + lane * 8, P_A + wv * 4096 + 3072);
    const int c2o = 16384 - 3072 - wv * 3584;
    float mx[3], sum[3];
    int um = 0;
#define UM() do { if (um < 15) { U[um % 3][um / 3] = mfma(f0.v[um % 3], f0.w[um / 3], (f32x4){0.f, 0.f, 0.f, 0.f}); ++um; } SB(); } while (0)
#pragma unroll
    for (int n = 0; n < 3; ++n)
      mx[n] = fmaxf(fmaxf(fmaxf(fmaxf(fmaxf(fmaxf(S[n][0][0], S[n][0][1]), S[n][0][2]), S[n][0][3]), S[n][1][0]), fmaxf(fmaxf(S[n][1][1], S[n][1][2]), S[n][1][3])),
                    fmaxf(fmaxf(fmaxf(S[n][2][0], S[n][2][1]), S[n][2][2]), S[n][2][3]));
#pragma unroll
    for (int n = 0; n < 3; ++n) { auto r = __builtin_amdgcn_permlane16_swap(__float_as_uint(mx[n]), __float_as_uint(mx[n]), false, false); mx[n] = fmaxf(__uint_as_float(r[0]), __uint_as_float(r[1])); }
#pragma unroll
    for (int n = 0; n < 3; ++n) { auto r = __builtin_amdgcn_permlane32_swap(__float_as_uint(mx[n]), __float_as_uint(mx[n]), false, false); mx[n] = fmaxf(__uint_as_float(r[0]), __uint_as_float(r[1])) * -0.12751743344863963f; }
    SB();
#pragma unroll
    for (int n = 0; n < 3; ++n)
#pragma unroll
      for (int kt = 0; kt < 3; ++kt) {
#pragma unroll
        for (int i = 0; i < 4; ++i) S[n][kt][i] = __builtin_amdgcn_exp2f(__builtin_fmaf(S[n][kt][i], 0.12751743344863963f, mx[n]));
        UM();
      }

#pragma unroll
    for (int n = 0; n < 3; ++n) { const f32x4 s4 = S[n][0] + S[n][1] + S[n][2]; sum[n] = (s4[0] + s4[1]) + (s4[2] + s4[3]); UM(); }
#pragma unroll
    for (int n = 0; n < 3; ++n) { auto r = __builtin_amdgcn_permlane16_swap(__float_as_uint(sum[n]), __float_as_uint(sum[n]), false, false); sum[n] = __uint_as_float(r[0]) + __uint_as_float(r[1]); }
#pragma unroll
    for (int n = 0; n < 3; ++n) { auto r = __builtin_amdgcn_permlane32_swap(__float_as_uint(sum[n]), __float_as_uint(sum[n]), false, false); sum[n] = __builtin_amdgcn_rcpf(__uint_as_float(r[0]) + __uint_as_float(r[1])); }
    SB();

#pragma unroll
    for (int n = 0; n < 3; ++n) {
      const u32x4 pa = cat(pk4(S[n][0] * sum[n]), pk4(S[n][1] * sum[n]));
      const u32x2 pc = pk4(S[n][2] * sum[n]);
      __builtin_amdgcn_raw_buffer_store_b128(pa, rp, pso + lane * 16 + n * 1024, 0, 16);
      __builtin_amdgcn_raw_buffer_store_b64(pc, rp, lane * 8 + 3072 + (n < 2 ? n * 512 : 0), pso + (n < 2 ? 0 : c2o), 16);
      *(LAS u32x4*)(pl + n * 1024) = pa;
      *(LAS u32x2*)(n < 2 ? pl2 + n * 512 : vadd(pl2, c2o)) = pc;
      UM();
    }
  }

  SB();
  u32x4 UA[5]; u32x2 UC[5];
  const bool xown = (wv == c.rank) && ((c.j >> 1) == (c.rank & 1));
#define ENDI(gi) do { if ((gi) == 10 || (gi) == 19) WAITV(0); if ((gi) < 22) LBARRIER(); SB(); } while (0)
#define SLOT(gi) ((((gi) - 9) & 1) * RP)
#define FIN(PE, CE, READ, FIRST, INB, off) do { const int l_ = lane_id(), pb_ = (INB) ? 28672 + bl * 2 * RP : P_A + bl * 8192; \
    finish2<PE, CE, READ, FIRST, (INB) ? RP : 4096>(O, U, UA, UC, f0, f1, a, off, vadd(sm + l_ * 16, pb_), vadd(sm + l_ * 8, pb_ + 3072), \
                                                   vadd(sm + l_ * 8, ((INB) ? P_B2 : P_A + 16384) + bl * 1024)); } while (0)
#define BODY(t, PE, CE) do { \
    constexpr int g0 = 9 + 4 * (t); \
    if ((t) > 0) { \
      FIN(PE, CE, true, (t) == 1, (t) == 3, SLOT(g0)); \
      if ((t) == 2) { \
        if (xown) { _Pragma("unroll") for (int n = 0; n < 3; ++n) *(LAS f32x4*)(sm + XL + n * 1024 + lane_id() * 16) = O[4][n]; } \
        else { const rsrc_t rxx = mk(ws + WS_X, 1024u * 2048u); \
          _Pragma("unroll") for (int n = 0; n < 3; ++n) __builtin_amdgcn_raw_buffer_store_b64(pk4(O[4][n]), rxx, lane_id() * 8 + n * 512, (cu * 4 + wv) * 2048u, 16); } \
      } \
      step2<CE, true, false, false, 0>(U, f0, f1, a, 0, 0, UA, UC); } \
    ENDI(g0); \
    step2<CE, false, true, false, 0>(U, f1, f0, a, SLOT(g0 + 1), 0, UA, UC); \
    step2<CE, false, true, false, 3, 2 * RP>(U, f0, f1, a, SLOT(g0 + 1), 1, UA, UC); ENDI(g0 + 1); \
    step2<CE, false, true, false, 0>(U, f1, f0, a, SLOT(g0 + 2), 0, UA, UC); \
    step2<CE, true, true, true, 3>(U, f0, f1, a, SLOT(g0 + 2), 1, UA, UC); ENDI(g0 + 2); \
    step2<CE, false, true, false, 0>(U, f1, f0, a, SLOT(g0 + 3), 0, UA, UC); \
    step2<CE, false, true, false, 3, -2 * RP>(U, f0, f1, a, SLOT(g0 + 3), 1, UA, UC); ENDI(g0 + 3); \
  } while (0)
  BODY(0, true, true);
  BODY(1, true, true);
  BODY(2, true, false);
  BODY(3, false, false);
  FIN(false, false, false, false, false, 0);

  tail(c, sm, wv, O);
}

__global__ __launch_bounds__(512, 2) void fused_attn_block(const char* xA, const char* kA, const char* vA, const char* wqA, const char* woA,
                                                          const char* xB, const char* kB, const char* vB, const char* wqB, const char* woB,
                                                          char* out, char* ws) {
  __shared__ __attribute__((aligned(256))) char smem[LDS_BYTES];
  LAS char* sm = (LAS char*)smem;
  const int wv = __builtin_amdgcn_readfirstlane(threadIdx.x >> 6);
  Ctx c;
  const int id = blockIdx.x;
  c.br = id & 1; c.rank = (id >> 1) & 3; c.j = (id >> 3) & 3; c.bp = id >> 5;
  c.h0 = c.rank * 8; c.kvsh = 1 - c.br;
  c.hp = (c.j + c.bp) & 3;
  { const int g3 = (c.bp + c.hp) % 3;
    c.ord = g3 == 0 ? 0x875462130ull : g3 == 1 ? 0x865732401ull : 0x768435102ull;
    c.kpos = g3 == 1 ? 2 : 1; }
  c.x = c.br ? xB : xA; c.k = c.br ? kB : kA; c.v = c.br ? vB : vA; c.wq = c.br ? wqB : wqA; c.wo = c.br ? woB : woA;
  c.out = out; c.ws = ws;
  asm volatile("s_getpc_b64 %0" : "=s"(c.pc));
  start(c, sm, lane_id(), wv);
  u64 tok = 0;
  { int w2 = wv; asm volatile("" : "+s"(w2));
    if (__builtin_expect(w2 >= 4, 1)) tok = helper1(c, sm, w2 - 4); }
  if (__builtin_expect(wv < 4, 1)) compute(c, sm, wv);
  else helper2(c, sm, wv - 4, tok);
}

extern "C" void kernel_launch(void* const* d_in, const int* in_sizes, int n_in, void* d_out, int out_size, void* d_ws, size_t ws_size,
                              hipStream_t stream) {
  const char* const* in = (const char* const*)d_in;
  fused_attn_block<<<256, 512, 0, stream>>>(in[0], in[1], in[2], in[3], in[4], in[5], in[6], in[7], in[8], in[9], (char*)d_out, (char*)d_ws);
}
```

```cpp
#include <hip/hip_runtime.h>
#include <stdint.h>

typedef unsigned u32;
typedef unsigned long long u64;
typedef u32 u32x4 __attribute__((ext_vector_type(4)));
typedef u32 u32x2 __attribute__((ext_vector_type(2)));
typedef float f32x4 __attribute__((ext_vector_type(4)));
typedef float f32x2 __attribute__((ext_vector_type(2)));
typedef short s16x4 __attribute__((ext_vector_type(4)));
typedef __bf16 bf16x8 __attribute__((ext_vector_type(8)));
typedef __bf16 bf16x2 __attribute__((ext_vector_type(2)));
#define LAS __attribute__((address_space(3)))
#define GAS __attribute__((address_space(1)))
#define DI __device__ __forceinline__
typedef __amdgpu_buffer_rsrc_t rsrc_t;

constexpr int QP = 45056;
constexpr int RP = 32768;
constexpr int KS = 135168;
constexpr int P_A = 131072;
constexpr int XL = 149504;
constexpr int P_B2 = 154880;
constexpr int OWN = 0;
constexpr int MISC = 153600;
constexpr int JUNK = 153856;
constexpr int LDS_BYTES = 156928;
constexpr u32 WS_F1 = 0;
constexpr u32 WS_F3 = 32768;
constexpr u32 WS_R = 65536;
constexpr u32 WS_P = 131072;
constexpr u32 WS_M = WS_P + 256u * 18432u;
constexpr u32 WS_X = WS_M + 1024u * 6144u;
constexpr u64 TOKK = 0x9E3779B97F4A7C15ull;
constexpr u32 SPIN_CAP = 1u << 20;
constexpr u32 CODE_KB = 32;

DI rsrc_t mk(const void* p, u32 bytes) { return __builtin_amdgcn_make_buffer_rsrc((void*)p, 0, bytes, 0x00020000); }
DI void dma_asm(rsrc_t r, u32 dst, u32 voff, u32 soff) {
  asm volatile("s_mov_b32 m0, %0\n\ts_nop 0\n\tbuffer_load_dwordx4 %1, %2, %3 offen lds" :: "s"(dst), "v"(voff), "s"(r), "s"(soff) : "memory", "m0");
}
template <int N, int STEP> DI void dma_run(rsrc_t r, u32 dst, u32 voff, u32 soff) {
  u32 t;
#define PIECE_ "s_add_u32 m0, m0, 0x1000\n\ts_add_u32 %0, %0, %5\n\tbuffer_load_dwordx4 %2, %3, %0 offen lds\n\t"
#define FIRST_ "s_mov_b32 m0, %1\n\ts_mov_b32 %0, %4\n\tbuffer_load_dwordx4 %2, %3, %0 offen lds\n\t"
  if (N == 3) asm volatile(FIRST_ PIECE_ PIECE_ : "=&s"(t) : "s"(dst), "v"(voff), "s"(r), "s"(soff), "n"(STEP) : "memory", "m0", "scc");
  if (N == 4) asm volatile(FIRST_ PIECE_ PIECE_ PIECE_ : "=&s"(t) : "s"(dst), "v"(voff), "s"(r), "s"(soff), "n"(STEP) : "memory", "m0", "scc");
}
#define WAITV(n) asm volatile("s_waitcnt vmcnt(%0)" ::"i"(n) : "memory")
#define BARRIER() asm volatile("s_barrier" ::: "memory")
#define LBARRIER() do { __builtin_amdgcn_s_waitcnt(0xC07F); asm volatile("s_barrier" ::: "memory"); } while (0)
#define SB() __builtin_amdgcn_sched_barrier(0)
#define PIN(x) asm volatile("" : "+v"(x))
DI int lane_id() { int l = __builtin_amdgcn_mbcnt_hi(~0u, __builtin_amdgcn_mbcnt_lo(~0u, 0u)); PIN(l); return l; }
DI f32x4 mfma(u32x4 a, u32x4 b, f32x4 c) { return __builtin_amdgcn_mfma_f32_16x16x32_bf16(__builtin_bit_cast(bf16x8, a), __builtin_bit_cast(bf16x8, b), c, 0, 0, 0); }
DI u32 pk(float a, float b) { f32x2 f = {a, b}; return __builtin_bit_cast(u32, __builtin_convertvector(f, bf16x2)); }
DI u32x2 pk4(f32x4 v) { u32x2 r = {pk(v[0], v[1]), pk(v[2], v[3])}; return r; }
DI u32x4 cat(u32x2 a, u32x2 b) { u32x4 r = {a[0], a[1], b[0], b[1]}; return r; }
DI u32x2 trd(LAS char* p) { return __builtin_bit_cast(u32x2, __builtin_amdgcn_ds_read_tr16_b64_v4i16((LAS s16x4*)p)); }
DI u32x4 lds128(LAS char* p) { return *(LAS u32x4*)p; }
DI u32x2 lds64(LAS char* p) { return *(LAS u32x2*)p; }
DI float bflo(u32 x) { return __builtin_bit_cast(float, x << 16); }
DI float bfhi(u32 x) { return __builtin_bit_cast(float, x & 0xffff0000u); }
DI u64 rdlane64(u64 v, int l) { return ((u64)(u32)__builtin_amdgcn_readlane((int)(v >> 32), l) << 32) | (u32)__builtin_amdgcn_readlane((int)v, l); }
DI LAS char* vadd(LAS char* p, int d) { u32 r; asm volatile("v_add_u32 %0, %1, %2" : "=v"(r) : "s"(d), "v"((u32)(uintptr_t)p)); return (LAS char*)(uintptr_t)r; }

struct Ctx {
  const char *x, *k, *v, *wq, *wo;
  u64 pc;
  char *out, *ws;
  u32 junk;
  u64 ord;
  int br, rank, bp, j;
  int h0, kvsh, hp, kpos;
};


DI u64 ld_async(const char* base, u32 off) { u64 r; asm volatile("global_load_dwordx2 %0, %1, %2 sc1" : "=v"(r) : "v"(off), "s"(base) : "memory"); return r; }
DI u64 ld_now(const char* base, u32 off) { u64 r; asm volatile("global_load_dwordx2 %0, %1, %2 sc1\n\ts_waitcnt vmcnt(0)" : "=&v"(r) : "v"(off), "s"(base) : "memory"); return r; }
DI void st_one(const char* base, u32 off, u64 v, u64 lanes) {
  asm volatile("s_mov_b64 exec, %3\n\tglobal_store_dwordx2 %0, %1, %2 sc1\n\ts_mov_b64 exec, -1" :: "v"(off), "v"(v), "s"(base), "s"(lanes) : "memory");
}

DI u64 helper1(Ctx& c, LAS char* sm, int l) {
  const int lane = lane_id(), hkv = 32 >> c.kvsh;
  const u32 base = (u32)(uintptr_t)sm;
  const rsrc_t rx = mk(c.x, 768 * 1152), rq = mk(c.wq, 576 * 8192), rk = mk(c.k, (u32)(16 * hkv) * (48 * 256));
  const u32 l4 = lane >> 4;
  const u32 vx = (lane >> 3) * 1152 + ((lane & 7) ^ (l4 | ((l & 1) << 2))) * 16;
  const u32 vq = l4 * 8192 + ((lane & 15) ^ ((l4 | ((l & 2) << 1)) << 1)) * 16;
  const u32 vk = l4 * 256 + ((lane & 15) ^ (l4 | (l << 2))) * 16;
  const u32 xs = (u32)(c.bp * 96 + l * 8) * 1152u, qs = (u32)(c.h0 + 2 * c.hp) * 256u + l * (4 * 8192);
  const u32 k0 = (u32)(2 * c.bp * hkv + ((c.h0 + 2 * c.hp) >> c.kvsh)) * (48 * 256) + l * 1024, k1 = c.kvsh ? 0u : 48u * 256u, k2 = (u32)hkv * (48 * 256);
  const int sg = c.br * 8 + c.bp, me = c.rank * 4 + c.j, m = lane & 15;
  u64 tok = 0;

#define STAGE(st_, slot_) do { const u32 k_ = (u32)(c.ord >> (4 * (st_))) & 15u, d_ = base + (slot_) * QP + l * 1024; \
    dma_run<3, 32 * 1152>(rx, d_, vx, xs + k_ * 128); \
    dma_run<4, 16 * 8192>(rq, d_ + 12288, vq, qs + k_ * (64 * 8192)); \
    dma_run<4, 16 * 8192>(rq, d_ + 28672, vq, qs + k_ * (64 * 8192) + 256); } while (0)
#define KTILE(kt_) do { _Pragma("unroll") for (int i = 0; i < 4; ++i) dma_asm(rk, base + KS + l * 1024 + i * 4096, vk, k0 + (i & 1) * k1 + (i >> 1) * k2 + (kt_) * 4096); } while (0)
  u32 junk = c.junk;
  {
    const u32 o0 = ((2 * c.bp + (l & 1)) * hkv + (c.h0 >> c.kvsh)) * (48 * 256), o1 = (c.h0 * 128 + l * 341) * 1152u, o2 = l * (191 * 8192) + c.h0 * 256;
    const u32 o3 = blockIdx.x * (l == 1 ? 18432u : l == 2 ? 24576u : l == 3 ? 8192u : 0u) + (l == 1 ? WS_P : l == 2 ? WS_M : l == 3 ? WS_X : WS_F1 + 64);
    const u32 o4 = ((2 * c.bp + (c.rank >> 1)) * 48 + l * 15) * 2304u + c.br * 1152 + c.j * 256 + (c.rank & 1) * 128;
    u32 j2;
    asm volatile("s_load_dword %0, %1, %6\n\ts_load_dword %0, %2, %7\n\ts_load_dword %0, %3, %8\n\ts_load_dword %0, %4, %9\n\ts_load_dword %0, %5, %10"
                 : "=&s"(j2) : "s"(l & 2 ? c.k : c.v), "s"(c.wo), "s"(c.wq), "s"(c.ws), "s"(c.out), "s"(o0), "s"(o1), "s"(o2), "s"(o3), "s"(o4) : "memory");
    junk = j2;
  }
  STAGE(1, 1);
  if (c.kpos == 1) { KTILE(0); WAITV(15); } else WAITV(11);
 BARRIER();
  { u32 line = ((((blockIdx.x >> 3) & 1) * 4 + l) * 64 + lane); line = line < CODE_KB * 8 ? line : 0u;
    asm volatile("s_mov_b32 m0, %0\n\ts_nop 0\n\tglobal_load_lds_dword %1, %2" :: "s"(base + JUNK), "v"(line * 128), "s"(c.pc & ~255ull) : "memory", "m0"); }
  tok = ld_async(c.ws + WS_F1 + sg * 2048, m * 128);
  STAGE(2, 2);
  if (c.kpos == 2) { KTILE(0); WAITV(15); } else WAITV(11);
  asm volatile("s_waitcnt lgkmcnt(0)" :: "s"(junk), "s"(c.junk) : "memory");
  PIN(tok); BARRIER();
  tok += TOKK;
  if (l == 3) {
    st_one(c.ws + WS_R + sg * 2048, m * 128 + me * 8, tok, 0xffffull);
    *(LAS u64*)(sm + MISC + m * 8) = tok; *(LAS u32*)(sm + MISC + 128) = 0u;
    asm volatile("s_waitcnt lgkmcnt(0)" ::: "memory");
  }
  int slot = 0, kt = 1;
#pragma unroll 1
  for (int st = 3; st < 9; ++st) {
    STAGE(st, slot);
    if (slot == c.kpos) { KTILE(kt); ++kt; WAITV(15); } else WAITV(11);
    slot = slot == 2 ? 0 : slot + 1;
    BARRIER();
  }
  return tok;
}

struct H2 {
  rsrc_t ro, rv;
  u32 vo, vv, ve, oso, eso, vs[3], base;
  int hown, h0, kvsh;
  template <bool EXT> DI void p2(int st) {
    const int h = h0 + ((hown + (st >> 1)) & 7);
    const u32 row = (u32)(h * 128 + (st & 1) * 64) * 1152u, vh = (u32)(h >> kvsh) * (48 * 256) + (st & 1) * 128, d = base + (st & 3) * RP;
#pragma unroll
    for (int i = 0; i < 3; ++i) dma_asm(rv, d + 16384 + i * 4096, vv, vs[i] + vh);
    dma_run<4, 16 * 1152>(ro, d, vo, oso + row);
    if (EXT) dma_asm(ro, d + 28672, ve, eso + row);
  }
};

DI void helper2(Ctx& c, LAS char* sm, int l, u64 tok) {
  const int lane = lane_id(), hkv = 32 >> c.kvsh, l4 = lane >> 4;
  H2 z;
  z.ro = mk(c.wo, 4096 * 1152); z.rv = mk(c.v, (u32)(16 * hkv) * (48 * 256));
  z.vo = l4 * 1152 + ((lane & 15) ^ ((l4 | ((l & 2) << 1)) << 1)) * 16;
  z.vv = (lane >> 3) * 256 + ((lane & 7) ^ (l4 | ((l & 1) << 2))) * 16;
  z.ve = (lane >> 2) * 1152 + ((lane & 3) ^ ((lane >> 5) << 1)) * 16;
  z.oso = c.j * 256 + l * (4 * 1152); z.eso = 1024 + (c.j & 1) * 64 + l * (16 * 1152);
  { const u32 b0 = (u32)(2 * c.bp * hkv) * (48 * 256), b1 = b0 + (u32)hkv * (48 * 256);
    z.vs[0] = b0 + l * 2048; z.vs[1] = l < 2 ? b0 + (l + 4) * 2048 : b1 + (l - 2) * 2048; z.vs[2] = b1 + (l + 2) * 2048; }
  z.base = (u32)(uintptr_t)sm + l * 1024; z.hown = 2 * c.hp; z.h0 = c.h0; z.kvsh = c.kvsh;
  const int sg = c.br * 8 + c.bp, me = c.rank * 4 + c.j;
  const char* F1 = c.ws + WS_F1 + sg * 2048;
  z.p2<true>(0); WAITV(8); BARRIER();
  z.p2<true>(1); WAITV(8); BARRIER();
  const u64 mytok = rdlane64(tok, me);
  const rsrc_t rp = mk(c.ws + WS_P, 256u * 18432u);
  const u32 p4 = 16384 + (l & 1) * 1024;
  LAS char* pl = sm + P_A + l * 1024 + lane * 16;
  LAS char* pl4 = sm + P_A + p4 + lane * 16;
  LAS char* pm = sm + 28672 + l * 1024 + lane * 16;
  LAS char* pm4 = sm + P_B2 + (l & 1) * 1024 + lane * 16;
  const int g = lane >> 4, m = lane & 15, hl = c.rank & 1;
  const bool xown = (c.j >> 1) == hl;
  const u32 cu = blockIdx.x, orow = (u32)((2 * c.bp + (c.rank >> 1)) * 48 + m) * 2304u + c.br * 1152;
  const bool need = m != me && (l < 3 ? (m & 3) == c.j : ((m ^ c.j) & 1) == 0);
  {
    u64 r = ld_async(c.ws + WS_R + sg * 2048, me * 128 + m * 8);
    z.p2<true>(2); z.p2<true>(3); WAITV(16); PIN(r);
    if (!__all(r == mytok))
      for (u32 spins = 0; spins < SPIN_CAP; ++spins) if (__all(ld_now(c.ws + WS_R + sg * 2048, me * 128 + m * 8) == mytok)) break;
  }
  BARRIER();
  z.p2<true>(4); WAITV(16); BARRIER();
  if (l == 3) st_one(F1, me * 128, mytok, 1ull);
  z.p2<true>(5); WAITV(16); BARRIER();

  z.p2<true>(6); WAITV(16); BARRIER();
#define PLOAD(pv, t, i) asm volatile("buffer_load_dwordx4 %0, %1, %2, %3 offen sc1" : "=v"(pv[i]) : "v"(lane * 16), "s"(rp), \
    "s"((u32)((c.bp * 4 + ((c.j + (t)) & 3)) * 8 + c.rank * 2 + c.br) * 18432u + ((i) < 4 ? l * 1024 + (i) * 4096 : p4)) : "memory")
#define PUTA(pv, i) do { PIN(pv[i]); if ((i) < 4 || l < 2) *(LAS u32x4*)((i) < 4 ? pl + (i) * 4096 : pl4) = pv[i]; } while (0)
#define PUTB(pv, i) do { PIN(pv[i]); if ((i) < 4 || l < 2) *(LAS u32x4*)((i) < 2 ? pm + (i) * RP : (i) < 4 ? pm + 2 * RP + ((i) - 2) * RP : pm4) = pv[i]; } while (0)
  u32x4 pa[5], pb[5], pc[5];
  {
    const u64 want = __shfl(tok, c.rank * 4 + (lane & 3));
    u64 r = ld_async(F1, (c.rank * 4 + (lane & 3)) * 128);
    z.p2<true>(7); WAITV(8); PIN(r);
    if (!__all(r == want))
      for (u32 spins = 0; spins < SPIN_CAP; ++spins) if (__all(ld_now(F1, (c.rank * 4 + (lane & 3)) * 128) == want)) break;
  }

  PLOAD(pa, 1, 0); PLOAD(pa, 1, 1); PLOAD(pa, 1, 2); PLOAD(pa, 1, 3); PLOAD(pa, 1, 4); WAITV(13); BARRIER();
  z.p2<false>(8); WAITV(7); PUTA(pa, 0); BARRIER();
  PUTA(pa, 1); PUTA(pa, 2); z.p2<false>(9); PLOAD(pb, 2, 0); PLOAD(pb, 2, 1); BARRIER();
  PUTA(pa, 3); PUTA(pa, 4); z.p2<false>(10); PLOAD(pb, 2, 2); WAITV(17); LBARRIER();
  PLOAD(pb, 2, 3); PLOAD(pb, 2, 4); z.p2<false>(11); WAITV(7);
  PUTB(pb, 0); PUTB(pb, 1); PUTB(pb, 2); PUTB(pb, 3); PUTB(pb, 4); BARRIER();
  z.p2<false>(12); PLOAD(pc, 3, 0); PLOAD(pc, 3, 1); BARRIER();
  z.p2<false>(13); PLOAD(pc, 3, 2); WAITV(17); BARRIER();
  if (l == 3) st_one(c.ws + WS_F3 + sg * 2048, me * 128 + 8, mytok, 1ull);
  z.p2<false>(14); PLOAD(pc, 3, 3); PLOAD(pc, 3, 4); WAITV(19); LBARRIER();
  z.p2<false>(15); WAITV(7);
  PUTA(pc, 0); PUTA(pc, 1); PUTA(pc, 2); PUTA(pc, 3); PUTA(pc, 4); WAITV(0); LBARRIER();
  BARRIER();
  if (l == 3 && !xown) return;
  for (u32 spins = 0; spins < SPIN_CAP; ++spins) if (__all(!need || ld_now(c.ws + WS_F3 + sg * 2048, m * 128 + (l == 3 ? 8 : 0)) == tok)) break;
  {
    const rsrc_t ro = mk(c.out, 768 * 2304);
    if (l < 3) {
      const rsrc_t rm = mk(c.ws + WS_M, 1024u * 6144u);
      u32x4 in[3][2];
#pragma unroll
      for (int r = 0; r < 3; ++r) {
        const u32 pcu = (cu & ~6u) | (((c.rank + 1 + r) & 3) << 1);
#pragma unroll
        for (int i = 0; i < 2; ++i) in[r][i] = __builtin_amdgcn_raw_buffer_load_b128(rm, lane * 16 + i * 1024, (pcu * 4 + c.rank) * 6144u + l * 2048, 16);
      }
#pragma unroll
      for (int c2 = 0; c2 < 2; ++c2) {
        f32x4 a = *(LAS f32x4*)(sm + OWN + (l * 4 + 2 * c2) * 1024 + lane * 16), d = *(LAS f32x4*)(sm + OWN + (l * 4 + 2 * c2 + 1) * 1024 + lane * 16);
#pragma unroll
        for (int r = 0; r < 3; ++r) {
          const u32x4 v = in[r][c2];
          a += (f32x4){bflo(v[0]), bfhi(v[0]), bflo(v[1]), bfhi(v[1])};
          d += (f32x4){bflo(v[2]), bfhi(v[2]), bflo(v[3]), bfhi(v[3])};
        }
        const u32x2 pa = pk4(a), pd = pk4(d);
        auto s0 = __builtin_amdgcn_permlane16_swap(pa[0], pd[0], false, false);
        auto s1 = __builtin_amdgcn_permlane16_swap(pa[1], pd[1], false, false);
        __builtin_amdgcn_raw_buffer_store_b128((u32x4){s0[0], s1[0], s0[1], s1[1]}, ro,
            orow + l * (16 * 2304) + (128 * c.j + 64 * hl + 32 * c2 + 16 * (g & 1) + 8 * (g >> 1)) * 2, 0, 0);
      }
    } else {
      const rsrc_t rxx = mk(c.ws + WS_X, 1024u * 2048u);
      u32x2 xin[7][3];
#pragma unroll
      for (int r = 0; r < 7; ++r) {
        const u32 pcu = ((cu & ~6u) | (((c.rank + 1 + r) & 3) << 1)) ^ (r >= 3 ? 16u : 0u);
#pragma unroll
        for (int n = 0; n < 3; ++n) xin[r][n] = __builtin_amdgcn_raw_buffer_load_b64(rxx, lane * 8 + n * 512, (pcu * 4 + c.rank) * 2048u, 16);
      }
#pragma unroll
      for (int n = 0; n < 3; ++n) {
        f32x4 a = *(LAS f32x4*)(sm + XL + n * 1024 + lane * 16);
#pragma unroll
        for (int r = 0; r < 7; ++r) a += (f32x4){bflo(xin[r][n][0]), bfhi(xin[r][n][0]), bflo(xin[r][n][1]), bfhi(xin[r][n][1])};
        __builtin_amdgcn_raw_buffer_store_b64(pk4(a), ro, orow + n * (16 * 2304) + (512 + 32 * (c.j & 1) + 16 * hl + 4 * g) * 2, 0, 0);
      }
    }
  }

}

template <bool ADV, int I0, int I1> DI void half1(f32x4 (&Q)[8][3], const u32x4 (&A)[8], const u32x4 (&B)[3], u32x4 (&An)[8], u32x4 (&Bn)[3],
                                  LAS char* (&w)[8], LAS char* (&x)[2], int u, int adv) {
  u32x2 lo[8];
#pragma unroll
  for (int i = I0; i < I1; ++i) {
    Q[i / 3][i % 3] = mfma(A[i / 3], B[i % 3], Q[i / 3][i % 3]);
    constexpr int what[19] = {70, 71, 00, 01, 100, 10, 11, 101, 20, 21, 102, 30, 31, 60, 40, 41, 61, 50, 51};
    if (i < 19) {
      const int k = what[i];
      if (k >= 100) Bn[k - 100] = lds128(x[u] + (k - 100) * 2048);
      else if (k % 10 == 0) lo[k / 10] = trd(w[k / 10] + u * 8192);
      else An[k / 10] = cat(lo[k / 10], trd(w[k / 10] + u * 8192 + 1024));
    } else if (ADV) {
      if (i < 23) { w[2 * (i - 19)] = vadd(w[2 * (i - 19)], adv); w[2 * (i - 19) + 1] = vadd(w[2 * (i - 19) + 1], adv); }
      else { x[0] = vadd(x[0], adv); x[1] = vadd(x[1], adv); }
    }
    SB();
  }
}
struct G2 { u32x4 v[3], w[5]; u32x2 lo; };
struct A2 { LAS char* w[4]; LAS char* e; LAS char* v[2]; };
template <bool EXT> DI void rd2(int i, G2& f, const A2& a, int off, int u) {
  if (i < 3) f.v[i] = lds128(a.v[u] + off + 16384 + i * 2048);
  else if (i < 11) { const int ct = (i - 3) >> 1; if ((i - 3) & 1) f.w[ct] = cat(f.lo, trd(a.w[ct] + off + u * 8192 + 1024)); else f.lo = trd(a.w[ct] + off + u * 8192); }
  else if (EXT && i == 11) f.lo = trd(a.e + off + 28672 + u * 2048);
  else if (EXT && i == 12) f.w[4] = cat(f.lo, trd(a.e + off + 28672 + u * 2048 + 256));
}
template <bool E, bool FIRST, bool READ, bool PACK, int DBL, int SH = 0>
DI void step2(f32x4 (&U)[3][5], const G2& f, G2& fn, A2& a, int off, int u, u32x4 (&UA)[5], u32x2 (&UC)[5]) {
  const f32x4 z = {0.f, 0.f, 0.f, 0.f};
#pragma unroll
  for (int i = 0; i < (E ? 15 : 12); ++i) {
    const int ct = i / 3, kt = i % 3;
    if (PACK) { const u32x2 t = pk4(U[kt][ct]); if (kt == 0) { UA[ct][0] = t[0]; UA[ct][1] = t[1]; } else if (kt == 1) { UA[ct][2] = t[0]; UA[ct][3] = t[1]; } else UC[ct] = t; SB(); }
    U[kt][ct] = mfma(f.v[kt], f.w[ct], FIRST ? z : U[kt][ct]);
    if (READ) { if (i < DBL) { rd2<E>(2 * i, fn, a, off, u); rd2<E>(2 * i + 1, fn, a, off, u); } else rd2<E>(i + DBL, fn, a, off, u); }
    if (SH) { const int k = i - (E ? 11 : 8);
      if (k == 0) { a.v[0] = vadd(a.v[0], SH); a.v[1] = vadd(a.v[1], SH); }
      if (k == 1) { a.w[0] = vadd(a.w[0], SH); a.w[1] = vadd(a.w[1], SH); }
      if (k == 2) { a.w[2] = vadd(a.w[2], SH); a.w[3] = vadd(a.w[3], SH); }
      if (k == 3) a.e = vadd(a.e, SH); }
    SB();
  }
}
template <bool PE, bool CE, bool READ, bool FIRST, int BS>
DI void finish2(f32x4 (&O)[5][3], f32x4 (&U)[3][5], const u32x4 (&A0)[5], const u32x2 (&C0)[5], G2& f0, G2& f1, const A2& a, int off, LAS char* p, LAS char* pc, LAS char* pc2) {
  constexpr int N = PE ? 15 : 12;
  u32x4 pa0[3], pa1[3], pcc[3], A1[5]; u32x2 C1[5], lo[3];
#pragma unroll
  for (int i = 0; i < N; ++i) {
    U[i % 3][i / 3] = mfma(f1.v[i % 3], f1.w[i / 3], U[i % 3][i / 3]);
    if (i < 3) pa0[i] = lds128(p + i * 1024);
    else if (i < 6) pa1[i - 3] = lds128(p + BS + (i - 3) * 1024);
    else if (READ) rd2<CE>(i - 6, f0, a, off, 0);
    SB();
  }
#pragma unroll
  for (int i = 0; i < N; ++i) {
    const int ct = i / 3, n = i % 3;
    O[ct][n] = mfma(A0[ct], pa0[n], FIRST ? (f32x4){0.f, 0.f, 0.f, 0.f} : O[ct][n]);
    const u32x2 t = pk4(U[n][ct]);
    if (n == 0) { A1[ct][0] = t[0]; A1[ct][1] = t[1]; } else if (n == 1) { A1[ct][2] = t[0]; A1[ct][3] = t[1]; } else C1[ct] = t;
    SB();
  }
#pragma unroll
  for (int i = 0; i < N; ++i) {
    O[i / 3][i % 3] = mfma(A1[i / 3], pa1[i % 3], O[i / 3][i % 3]);
    if (i < 3) lo[i] = lds64(i < 2 ? pc + i * 512 : pc2);
    else if (i < 6) pcc[i - 3] = cat(lo[i - 3], lds64(i < 5 ? pc + BS + (i - 3) * 512 : pc2 + 512));
    else if (READ) rd2<CE>(i + N - 12, f0, a, off, 0);
    SB();
  }
  if (READ && N == 12) rd2<CE>(12, f0, a, off, 0);
#pragma unroll
  for (int i = 0; i < N; ++i) {
    O[i / 3][i % 3] = mfma(cat(C0[i / 3], C1[i / 3]), pcc[i % 3], O[i / 3][i % 3]);
    if (READ) rd2<CE>(i, f1, a, off, 1);
    SB();
  }
  if (READ && N == 12) rd2<CE>(12, f1, a, off, 1);
}

DI void tail(Ctx& c, LAS char* sm, int wv, f32x4 (&O)[5][3]) {
  const int lane = lane_id();
  if (wv == c.rank) {
#pragma unroll
    for (int n = 0; n < 3; ++n)
#pragma unroll
      for (int ct = 0; ct < 4; ++ct) *(LAS f32x4*)(sm + OWN + (n * 4 + ct) * 1024 + lane * 16) = O[ct][n];
    LBARRIER();
    return;
  }
  const rsrc_t rm = mk(c.ws + WS_M, 1024u * 6144u);
#pragma unroll
  for (int n = 0; n < 3; ++n)
#pragma unroll
    for (int c2 = 0; c2 < 2; ++c2) __builtin_amdgcn_raw_buffer_store_b128(cat(pk4(O[2 * c2][n]), pk4(O[2 * c2 + 1][n])), rm, (blockIdx.x * 4 + wv) * 6144u + lane * 16 + (n * 2 + c2) * 1024, 0, 16);
  const int me = c.rank * 4 + c.j;
  const u64 tok = *(LAS u64*)(sm + MISC + me * 8);
  BARRIER();
  WAITV(0);

  u32 old;
  asm volatile("s_mov_b64 exec, 1\n\tds_add_rtn_u32 %0, %1, %2\n\ts_waitcnt lgkmcnt(0)\n\ts_mov_b64 exec, -1" : "=&v"(old) : "v"((u32)(uintptr_t)sm + MISC + 128), "v"(1u) : "memory");
  if (__builtin_amdgcn_readfirstlane(old) == 2u) st_one(c.ws + WS_F3 + (c.br * 8 + c.bp) * 2048, me * 128, tok, 1ull);
}

DI void compute(Ctx& c, LAS char* sm, int wv) {
  char* ws = c.ws;
  const int bl = wv >> 1, hl = wv & 1;
  const u32 cu = blockIdx.x;
  int lane = lane_id(), g = lane >> 4, m = lane & 15, q = m >> 2, p = lane & 3;
  u32 wbase = (8 * g + q) * 256 + 32 * (q | ((g & 1) << 2)) + 8 * p;
  u32 xbase = (bl * 48 + m) * 128 + ((g ^ (m >> 1)) * 16);
  u32x4 Kf[3][4];
  f32x4 Q[8][3];
  u32x4 A1[8], B1[3];
  LAS char* aw[8]; LAS char* ax[2];
#pragma unroll
  for (int T = 0; T < 8; ++T) { A1[T] = (u32x4){0, 0, 0, 0}; PIN(A1[T]);
    aw[T] = vadd(sm + (wbase ^ (T << 5)), 12288 + hl * 16384);
#pragma unroll
    for (int n = 0; n < 3; ++n) { Q[T][n] = (f32x4){0.f, 0.f, 0.f, 0.f}; PIN(Q[T][n]); } }
#pragma unroll
  for (int n = 0; n < 3; ++n) { B1[n] = (u32x4){0, 0, 0, 0}; PIN(B1[n]); }
  ax[0] = vadd(sm + xbase, 0); ax[1] = vadd(sm + (xbase ^ 64), 0);
  SB();
 asm volatile("s_waitcnt vmcnt(0) lgkmcnt(0)" :: "s"(c.junk) : "memory"); BARRIER();

#pragma unroll
  for (int kt = 0; kt < 3; ++kt) {
#pragma unroll 1
    for (int s = 0; s < 3; ++s) {
      u32x4 A0[8], B0[3];
      half1<false, 0, 20>(Q, A1, B1, A0, B0, aw, ax, 0, 0);
      if (s == c.kpos) {
        const u32 l = lane_id(), kb = KS + wv * 4096 + (l & 15) * 256 + ((l >> 5) ^ (l & 15)) * 16 + ((l >> 1) & 8);
#pragma unroll
        for (int i = 0; i < 4; ++i) Kf[kt][i] = cat(lds64(sm + (kb ^ (i << 6))), lds64(sm + (kb ^ (i << 6) ^ 32)));
      }
      half1<false, 20, 24>(Q, A1, B1, A0, B0, aw, ax, 0, 0);
      half1<true, 0, 24>(Q, A0, B0, A1, B1, aw, ax, 1, s == 2 ? -2 * QP : QP);
      LBARRIER();

    }
  }
  SB();
  lane = lane_id(); g = lane >> 4; m = lane & 15; q = m >> 2; p = lane & 3;
  wbase = (8 * g + q) * 256 + 32 * (q | ((g & 1) << 2)) + 8 * p;
  xbase = (bl * 48 + m) * 128 + ((g ^ (m >> 1)) * 16);
  A2 a;
#pragma unroll
  for (int ct = 0; ct < 4; ++ct) a.w[ct] = sm + (wbase ^ ((4 * hl + ct) << 5));
  a.e = sm + (8 * g + q) * 64 + 32 * (hl ^ (g & 1)) + 8 * p;
  a.v[0] = sm + xbase; a.v[1] = sm + (xbase ^ 64);
  G2 f0, f1;
  f32x4 O[5][3], U[3][5];
  {
    u32x4 qf[3][4];
    f32x4 S[3][3];
#pragma unroll
    for (int i = 0; i < 24; ++i) {
      Q[i & 7][i >> 3] = mfma(A1[i & 7], B1[i >> 3], Q[i & 7][i >> 3]);
      if (i >= 8) { const u32x2 t = pk4(Q[i & 7][(i >> 3) - 1]); qf[(i >> 3) - 1][(i & 7) >> 1][2 * (i & 1)] = t[0]; qf[(i >> 3) - 1][(i & 7) >> 1][2 * (i & 1) + 1] = t[1]; }
      SB();
    }

#pragma unroll
    for (int i = 0; i < 36; ++i) {
      const int n = i / 12, s = (i % 12) / 3, kt = i % 3;
      S[n][kt] = mfma(Kf[kt][s], qf[n][s], s == 0 ? (f32x4){0.f, 0.f, 0.f, 0.f} : S[n][kt]);
      if (i >= 2 && i < 10) { const u32x2 t = pk4(Q[i - 2][2]); qf[2][(i - 2) >> 1][2 * (i & 1)] = t[0]; qf[2][(i - 2) >> 1][2 * (i & 1) + 1] = t[1]; }
      if (i >= 10 && i < 23) rd2<true>(i - 10, f0, a, 0, 0);
      if (i >= 23) rd2<true>(i - 23, f1, a, 0, 1);
      SB();
    }

    const rsrc_t rp = mk(ws + WS_P, 256u * 18432u);
    const u32 pso = cu * 18432u + wv * 4096;
    LAS char* pl = vadd(sm + lane * 16, P_A + wv * 4096);
    LAS char* pl2 = vadd(sm + lane * 8, P_A + wv * 4096 + 3072);
    const int c2o = 16384 - 3072 - wv * 3584;
    float mx[3], sum[3];
    int um = 0;
#define UM() do { if (um < 15) { U[um % 3][um / 3] = mfma(f0.v[um % 3], f0.w[um / 3], (f32x4){0.f, 0.f, 0.f, 0.f}); ++um; } SB(); } while (0)
#pragma unroll
    for (int n = 0; n < 3; ++n)
      mx[n] = fmaxf(fmaxf(fmaxf(fmaxf(fmaxf(fmaxf(S[n][0][0], S[n][0][1]), S[n][0][2]), S[n][0][3]), S[n][1][0]), fmaxf(fmaxf(S[n][1][1], S[n][1][2]), S[n][1][3])),
                    fmaxf(fmaxf(fmaxf(S[n][2][0], S[n][2][1]), S[n][2][2]), S[n][2][3]));
#pragma unroll
    for (int n = 0; n < 3; ++n) { auto r = __builtin_amdgcn_permlane16_swap(__float_as_uint(mx[n]), __float_as_uint(mx[n]), false, false); mx[n] = fmaxf(__uint_as_float(r[0]), __uint_as_float(r[1])); }
#pragma unroll
    for (int n = 0; n < 3; ++n) { auto r = __builtin_amdgcn_permlane32_swap(__float_as_uint(mx[n]), __float_as_uint(mx[n]), false, false); mx[n] = fmaxf(__uint_as_float(r[0]), __uint_as_float(r[1])) * -0.12751743344863963f; }
    SB();
#pragma unroll
    for (int n = 0; n < 3; ++n)
#pragma unroll
      for (int kt = 0; kt < 3; ++kt) {
#pragma unroll
        for (int i = 0; i < 4; ++i) S[n][kt][i] = __builtin_amdgcn_exp2f(__builtin_fmaf(S[n][kt][i], 0.12751743344863963f, mx[n]));
        UM();
      }

#pragma unroll
    for (int n = 0; n < 3; ++n) { const f32x4 s4 = S[n][0] + S[n][1] + S[n][2]; sum[n] = (s4[0] + s4[1]) + (s4[2] + s4[3]); UM(); }
#pragma unroll
    for (int n = 0; n < 3; ++n) { auto r = __builtin_amdgcn_permlane16_swap(__float_as_uint(sum[n]), __float_as_uint(sum[n]), false, false); sum[n] = __uint_as_float(r[0]) + __uint_as_float(r[1]); }
#pragma unroll
    for (int n = 0; n < 3; ++n) { auto r = __builtin_amdgcn_permlane32_swap(__float_as_uint(sum[n]), __float_as_uint(sum[n]), false, false); sum[n] = __builtin_amdgcn_rcpf(__uint_as_float(r[0]) + __uint_as_float(r[1])); }
    SB();

#pragma unroll
    for (int n = 0; n < 3; ++n) {
      const u32x4 pa = cat(pk4(S[n][0] * sum[n]), pk4(S[n][1] * sum[n]));
      const u32x2 pc = pk4(S[n][2] * sum[n]);
      __builtin_amdgcn_raw_buffer_store_b128(pa, rp, pso + lane * 16 + n * 1024, 0, 16);
      __builtin_amdgcn_raw_buffer_store_b64(pc, rp, lane * 8 + 3072 + (n < 2 ? n * 512 : 0), pso + (n < 2 ? 0 : c2o), 16);
      *(LAS u32x4*)(pl + n * 1024) = pa;
      *(LAS u32x2*)(n < 2 ? pl2 + n * 512 : vadd(pl2, c2o)) = pc;
      UM();
    }
  }

  SB();
  u32x4 UA[5]; u32x2 UC[5];
  const bool xown = (wv == c.rank) && ((c.j >> 1) == (c.rank & 1));
#define ENDI(gi) do { if ((gi) == 10 || (gi) == 19) WAITV(0); if ((gi) < 22) LBARRIER(); SB(); } while (0)
#define SLOT(gi) ((((gi) - 9) & 1) * RP)
  LAS char *pp0, *pp1, *pp2;
#define PPTR(INB) do { const int l_ = lane_id(), pb_ = (INB) ? 28672 + bl * 2 * RP : P_A + bl * 8192; \
    pp0 = vadd(sm + l_ * 16, pb_); pp1 = vadd(sm + l_ * 8, pb_ + 3072); pp2 = vadd(sm + l_ * 8, ((INB) ? P_B2 : P_A + 16384) + bl * 1024); SB(); } while (0)
#define FIN(PE, CE, READ, FIRST, INB, off) finish2<PE, CE, READ, FIRST, (INB) ? RP : 4096>(O, U, UA, UC, f0, f1, a, off, pp0, pp1, pp2)
#define BODY(t, PE, CE) do { \
    constexpr int g0 = 9 + 4 * (t); \
    if ((t) > 0) { \
      FIN(PE, CE, true, (t) == 1, (t) == 3, SLOT(g0)); \
      if ((t) == 2) { \
        if (xown) { _Pragma("unroll") for (int n = 0; n < 3; ++n) *(LAS f32x4*)(sm + XL + n * 1024 + lane_id() * 16) = O[4][n]; } \
        else { const rsrc_t rxx = mk(ws + WS_X, 1024u * 2048u); \
          _Pragma("unroll") for (int n = 0; n < 3; ++n) __builtin_amdgcn_raw_buffer_store_b64(pk4(O[4][n]), rxx, lane_id() * 8 + n * 512, (cu * 4 + wv) * 2048u, 16); } \
      } \
      step2<CE, true, false, false, 0>(U, f0, f1, a, 0, 0, UA, UC); } \
    ENDI(g0); \
    step2<CE, false, true, false, 0>(U, f1, f0, a, SLOT(g0 + 1), 0, UA, UC); \
    step2<CE, false, true, false, 3, 2 * RP>(U, f0, f1, a, SLOT(g0 + 1), 1, UA, UC); ENDI(g0 + 1); \
    step2<CE, false, true, false, 0>(U, f1, f0, a, SLOT(g0 + 2), 0, UA, UC); \
    step2<CE, true, true, true, 3>(U, f0, f1, a, SLOT(g0 + 2), 1, UA, UC); ENDI(g0 + 2); \
    step2<CE, false, true, false, 0>(U, f1, f0, a, SLOT(g0 + 3), 0, UA, UC); \
    step2<CE, false, true, false, 3, -2 * RP>(U, f0, f1, a, SLOT(g0 + 3), 1, UA, UC); PPTR((t) == 2); ENDI(g0 + 3); \
  } while (0)
  BODY(0, true, true);
  BODY(1, true, true);
  BODY(2, true, false);
  BODY(3, false, false);
  FIN(false, false, false, false, false, 0);

  tail(c, sm, wv, O);
}

__global__ __launch_bounds__(512, 2) void fused_attn_block(const char* xA, const char* kA, const char* vA, const char* wqA, const char* woA,
                                                          const char* xB, const char* kB, const char* vB, const char* wqB, const char* woB,
                                                          char* out, char* ws) {
  __shared__ __attribute__((aligned(256))) char smem[LDS_BYTES];
  LAS char* sm = (LAS char*)smem;
  const int wv = __builtin_amdgcn_readfirstlane(threadIdx.x >> 6);
  Ctx c;
  const int id = blockIdx.x;
  c.br = id & 1; c.rank = (id >> 1) & 3; c.j = (id >> 3) & 3; c.bp = id >> 5;
  c.h0 = c.rank * 8; c.kvsh = 1 - c.br;
  c.hp = (c.j + c.bp) & 3;
  { const int g3 = (c.bp + c.hp) % 3;
    c.ord = g3 == 0 ? 0x875462130ull : g3 == 1 ? 0x865732401ull : 0x768435102ull;
    c.kpos = g3 == 1 ? 2 : 1; }
  {
    const u32 lane = lane_id(), l4 = lane >> 4, k0 = (u32)c.ord & 15u;
    u32 vx = (lane >> 3) * 1152 + ((lane & 7) ^ (l4 | ((wv & 1) << 2))) * 16;
    u32 vq = l4 * 8192 + ((lane & 15) ^ ((l4 | ((wv & 2) << 1)) << 1)) * 16;
    u32 xs = (u32)(c.bp * 96 + wv * 8) * 1152u + k0 * 128, qs = (u32)(c.h0 + 2 * c.hp) * 256u + wv * (4 * 8192) + k0 * (64 * 8192);
    u32 d = (u32)(uintptr_t)sm + wv * 1024;
    u32 d5 = wv < 4 ? d + 8192 : (u32)(uintptr_t)sm + JUNK, x5 = wv < 4 ? xs + 64 * 1152 : 0x7f000000u;
    asm volatile("s_getpc_b64 %0" : "=s"(c.pc));
    asm volatile("" : "+v"(vx), "+v"(vq), "+s"(xs), "+s"(qs), "+s"(d), "+s"(d5), "+s"(x5));
    SB();
    c.x = c.br ? xB : xA; c.k = c.br ? kB : kA; c.v = c.br ? vB : vA; c.wq = c.br ? wqB : wqA; c.wo = c.br ? woB : woA;
    c.out = out; c.ws = ws;
    u32 junk = 0;
    asm volatile("s_load_dword %0, %1, %2" : "+s"(junk) : "s"(c.wq), "s"(qs) : "memory");
    const rsrc_t rx = mk(c.x, 768 * 1152), rq = mk(c.wq, 576 * 8192);
    dma_asm(rx, d, vx, xs);
#pragma unroll
    for (int i = 0; i < 4; ++i) dma_asm(rq, d + 12288 + i * 8192, vq, qs + (i & 1) * (32 * 8192) + (i >> 1) * 256);
    dma_asm(rx, d5, vx, x5);
    c.junk = junk;

  }
  u64 tok = 0;
  { int w2 = wv; asm volatile("" : "+s"(w2));
    if (__builtin_expect(w2 >= 4, 1)) tok = helper1(c, sm, w2 - 4); }
  if (__builtin_expect(wv < 4, 1)) compute(c, sm, wv);
  else helper2(c, sm, wv - 4, tok);
}

extern "C" void kernel_launch(void* const* d_in, const int* in_sizes, int n_in, void* d_out, int out_size, void* d_ws, size_t ws_size,
                              hipStream_t stream) {
  const char* const* in = (const char* const*)d_in;
  fused_attn_block<<<256, 512, 0, stream>>>(in[0], in[1], in[2], in[3], in[4], in[5], in[6], in[7], in[8], in[9], (char*)d_out, (char*)d_ws);
}
```

```cpp
#define AMP_W 0
#define AMP_A 63
#define AMP_B 30
#include <hip/hip_runtime.h>
#include <stdint.h>

typedef unsigned u32;
typedef unsigned long long u64;
typedef u32 u32x4 __attribute__((ext_vector_type(4)));
typedef u32 u32x2 __attribute__((ext_vector_type(2)));
typedef u32 u32x8 __attribute__((ext_vector_type(8)));
typedef float f32x4 __attribute__((ext_vector_type(4)));
typedef float f32x2 __attribute__((ext_vector_type(2)));
typedef short s16x4 __attribute__((ext_vector_type(4)));
typedef __bf16 bf16x8 __attribute__((ext_vector_type(8)));
typedef __bf16 bf16x2 __attribute__((ext_vector_type(2)));
#define LAS __attribute__((address_space(3)))
#define GAS __attribute__((address_space(1)))
#define DI __device__ __forceinline__
typedef __amdgpu_buffer_rsrc_t rsrc_t;

#define DIAG 0
#define CODEPF 32
#define XB 11
#define ROTH 1
#define ROTK 4
#define PDMA 0
#define H3LATE 0
#define HPRIO 0
#define KPOS 0
#define PPRIO 0
#define SENT 0
#define SDELAY 12
#define CPRIO 0
#define STLB 3
#define NODRY 1
#define ABL_NOLOAD 0
#define ABL_NOCOMP 0

constexpr int QP = 45056;
constexpr int RP = 32768;
constexpr int KS = 135168;
constexpr int P_A = 131072;
constexpr int XL = 149504;
constexpr int P_B2 = 155904;
constexpr int OWN = 0;
constexpr int MISC = 153600;
constexpr int JUNK = 153856;
constexpr int TOKL = 154880;
constexpr int LDS_BYTES = 157952;
constexpr u32 WS_F1 = 0;
constexpr u32 WS_F3 = 32768;
constexpr u32 WS_R = 65536;
constexpr u32 WS_P = 131072;
constexpr u32 WS_M = WS_P + 256u * 18432u;
constexpr u32 WS_X = WS_M + 1024u * 6144u;
constexpr u32 WS_DIAG = 16u << 20;
constexpr u64 TOKK = 0x9E3779B97F4A7C15ull;
constexpr u32 SPIN_CAP = 1u << 20;

DI rsrc_t mk(const void* p, u32 bytes) { return __builtin_amdgcn_make_buffer_rsrc((void*)p, 0, bytes, 0x00020000); }
DI void dma(rsrc_t r, LAS char* dst, u32 voff, u32 soff) { if (ABL_NOLOAD) return; __builtin_amdgcn_raw_ptr_buffer_load_lds(r, (LAS void*)dst, 16, voff, soff, 0, 0); }
DI void dma_asm(rsrc_t r, u32 dst, u32 voff, u32 soff) {
  if (ABL_NOLOAD) return;
  asm volatile("s_mov_b32 m0, %0\n\ts_nop 0\n\tbuffer_load_dwordx4 %1, %2, %3 offen lds" :: "s"(dst), "v"(voff), "s"(r), "s"(soff) : "memory", "m0");
}
template <int N, int STEP> DI void dma_run(rsrc_t r, u32 dst, u32 voff, u32 soff) {
  if (ABL_NOLOAD) return;
  u32 t;
#define PIECE_ "s_add_u32 m0, m0, 0x1000\n\ts_add_u32 %0, %0, %5\n\tbuffer_load_dwordx4 %2, %3, %0 offen lds\n\t"
#define FIRST_ "s_mov_b32 m0, %1\n\ts_mov_b32 %0, %4\n\tbuffer_load_dwordx4 %2, %3, %0 offen lds\n\t"
  if (N == 2) asm volatile(FIRST_ PIECE_ : "=&s"(t) : "s"(dst), "v"(voff), "s"(r), "s"(soff), "n"(STEP) : "memory", "m0", "scc");
  if (N == 3) asm volatile(FIRST_ PIECE_ PIECE_ : "=&s"(t) : "s"(dst), "v"(voff), "s"(r), "s"(soff), "n"(STEP) : "memory", "m0", "scc");
  if (N == 4) asm volatile(FIRST_ PIECE_ PIECE_ PIECE_ : "=&s"(t) : "s"(dst), "v"(voff), "s"(r), "s"(soff), "n"(STEP) : "memory", "m0", "scc");
}
#define WAITV(n) asm volatile("s_waitcnt vmcnt(%0)" ::"i"(n) : "memory")
#define BARRIER() asm volatile("s_barrier" ::: "memory")
#define LBARRIER() do { __builtin_amdgcn_s_waitcnt(0xC07F); asm volatile("s_barrier" ::: "memory"); } while (0)
#define SB() __builtin_amdgcn_sched_barrier(0)
#define PIN(x) asm volatile("" : "+v"(x))
DI int lane_id() { int l = __builtin_amdgcn_mbcnt_hi(~0u, __builtin_amdgcn_mbcnt_lo(~0u, 0u)); PIN(l); return l; }
DI u32 vmcnt_now() { u32 v; asm volatile("s_getreg_b32 %0, hwreg(HW_REG_IB_STS)" : "=s"(v) :: "memory"); return v & 0x00C0000Fu; }
DI f32x4 mfma(u32x4 a, u32x4 b, f32x4 c) { return __builtin_amdgcn_mfma_f32_16x16x32_bf16(__builtin_bit_cast(bf16x8, a), __builtin_bit_cast(bf16x8, b), c, 0, 0, 0); }
DI u32 pk(float a, float b) { f32x2 f = {a, b}; return __builtin_bit_cast(u32, __builtin_convertvector(f, bf16x2)); }
DI u32x2 pk4(f32x4 v) { u32x2 r = {pk(v[0], v[1]), pk(v[2], v[3])}; return r; }
DI u32x4 cat(u32x2 a, u32x2 b) { u32x4 r = {a[0], a[1], b[0], b[1]}; return r; }
DI u32x2 trd(LAS char* p) { return __builtin_bit_cast(u32x2, __builtin_amdgcn_ds_read_tr16_b64_v4i16((LAS s16x4*)p)); }
DI u32x4 lds128(LAS char* p) { return *(LAS u32x4*)p; }
DI u32x2 lds64(LAS char* p) { return *(LAS u32x2*)p; }
DI float bflo(u32 x) { return __builtin_bit_cast(float, x << 16); }
DI float bfhi(u32 x) { return __builtin_bit_cast(float, x & 0xffff0000u); }
DI u64 ld_agent(GAS u64* p) { return __hip_atomic_load(p, __ATOMIC_RELAXED, __HIP_MEMORY_SCOPE_AGENT); }
DI void st_agent(GAS u64* p, u64 v) { __hip_atomic_store(p, v, __ATOMIC_RELAXED, __HIP_MEMORY_SCOPE_AGENT); }
DI u64 rdlane64(u64 v, int l) { return ((u64)(u32)__builtin_amdgcn_readlane((int)(v >> 32), l) << 32) | (u32)__builtin_amdgcn_readlane((int)v, l); }
DI LAS char* vadd(LAS char* p, int d) { u32 r; asm volatile("v_add_u32 %0, %1, %2" : "=v"(r) : "s"(d), "v"((u32)(uintptr_t)p)); return (LAS char*)(uintptr_t)r; }
DI int wlane(int val, int idx, int old) { asm volatile("s_mov_b32 m0, %2\n\ts_nop 0\n\tv_writelane_b32 %0, %1, m0" : "+v"(old) : "s"(val), "s"(idx) : "m0"); return old; }
#define STAMP(i) do { const int i_ = (i); if (i_ == AMP_A || i_ == AMP_B) { u64 t_; asm volatile("s_memrealtime %0\n\ts_waitcnt lgkmcnt(0)" : "=s"(t_) :: "memory"); if (i_ == AMP_A) c.ta = (u32)t_; else c.tb = (u32)t_; } } while (0)

struct Ctx {
  const char *x, *k, *v, *wq, *wo;
  u64 pc;
  char *out, *ws;
  u32 junk;
  int stamps; u32 ta, tb;
  int br, rank, bp, j, h0, kvsh, hp, rot, kpos; u64 ord;
  int dummy_;
};

DI u64 ld_async(const char* base, u32 off) { u64 r; asm volatile("global_load_dwordx2 %0, %1, %2 sc1" : "=v"(r) : "v"(off), "s"(base) : "memory"); return r; }
DI u64 ld_now(const char* base, u32 off) { u64 r; asm volatile("global_load_dwordx2 %0, %1, %2 sc1\n\ts_waitcnt vmcnt(0)" : "=&v"(r) : "v"(off), "s"(base) : "memory"); return r; }
DI void st_one(const char* base, u32 off, u64 v, u64 lanes) {
  asm volatile("s_mov_b64 exec, %3\n\tglobal_store_dwordx2 %0, %1, %2 sc1\n\ts_mov_b64 exec, -1" :: "v"(off), "v"(v), "s"(base), "s"(lanes) : "memory");
}

DI u64 helper1(Ctx& c, LAS char* sm, int l) {
  const int lane = lane_id(), hkv = 32 >> c.kvsh;
  const u32 base = (u32)(uintptr_t)sm;
  const rsrc_t rx = mk(c.x, 768 * 1152), rq = mk(c.wq, 576 * 8192), rk = mk(c.k, (u32)(16 * hkv) * (48 * 256));
  const u32 l4 = lane >> 4;
  const u32 vx = (lane >> 3) * 1152 + ((lane & 7) ^ (l4 | ((l & 1) << 2))) * 16;
  const u32 vq = l4 * 8192 + ((lane & 15) ^ ((l4 | ((l & 2) << 1)) << 1)) * 16;
  const u32 vk = l4 * 256 + ((lane & 15) ^ (l4 | (l << 2))) * 16;
  const u32 xs = (u32)(c.bp * 96 + l * 8) * 1152u, qs = (u32)(c.h0 + 2 * c.hp) * 256u + l * (4 * 8192);
  const u32 k0 = (u32)(2 * c.bp * hkv + ((c.h0 + 2 * c.hp) >> c.kvsh)) * (48 * 256) + l * 1024, k1 = c.kvsh ? 0u : 48u * 256u, k2 = (u32)hkv * (48 * 256);
  const int sg = c.br * 8 + c.bp, me = c.rank * 4 + c.j, m = lane & 15;
  u64 tok = 0;
  if (HPRIO) __builtin_amdgcn_s_setprio(HPRIO);
  STAMP(33);
#define STAGE(st_, slot_) do { const u32 k_ = (u32)(c.ord >> (4 * (st_))) & 15u, d_ = base + (slot_) * QP + l * 1024; \
    dma_run<3, 32 * 1152>(rx, d_, vx, xs + k_ * 128); \
    dma_run<4, 16 * 8192>(rq, d_ + 12288, vq, qs + k_ * (64 * 8192)); \
    dma_run<4, 16 * 8192>(rq, d_ + 28672, vq, qs + k_ * (64 * 8192) + 256); } while (0)
#define KTILE(kt_) do { _Pragma("unroll") for (int i = 0; i < 4; ++i) dma_asm(rk, base + KS + l * 1024 + i * 4096, vk, k0 + (i & 1) * k1 + (i >> 1) * k2 + (kt_) * 4096); } while (0)
  u32 junk = c.junk;
  if (STLB) {
    const u32 o0 = ((2 * c.bp + (l & 1)) * hkv + (c.h0 >> c.kvsh)) * (48 * 256), o1 = (c.h0 * 128 + l * 341) * 1152u, o2 = l * (191 * 8192) + c.h0 * 256;
    const u32 o3 = blockIdx.x * (l == 1 ? 18432u : l == 2 ? 24576u : l == 3 ? 8192u : 0u) + (l == 1 ? WS_P : l == 2 ? WS_M : l == 3 ? WS_X : WS_F1 + 64);
    const u32 o4 = ((2 * c.bp + (c.rank >> 1)) * 48 + l * 15) * 2304u + c.br * 1152 + c.j * 256 + (c.rank & 1) * 128;
    u32 j2;
    asm volatile("s_load_dword %0, %1, %6\n\ts_load_dword %0, %2, %7\n\ts_load_dword %0, %3, %8\n\ts_load_dword %0, %4, %9\n\ts_load_dword %0, %5, %10"
                 : "=&s"(j2) : "s"(l & 2 ? c.k : c.v), "s"(c.wo), "s"(c.wq), "s"(c.ws), "s"(c.out), "s"(o0), "s"(o1), "s"(o2), "s"(o3), "s"(o4) : "memory");
    junk = j2;
  }
  STAGE(1, 1);
  if (c.kpos == 1) { KTILE(0); WAITV(15); } else WAITV(11);
  STAMP(34); BARRIER();
  { u32 line = ((((blockIdx.x >> 3) & 1) * 4 + l) * 64 + lane); line = line < CODEPF * 8 ? line : 0u;
    asm volatile("s_mov_b32 m0, %0\n\ts_nop 0\n\tglobal_load_lds_dword %1, %2" :: "s"(base + JUNK), "v"(line * 128), "s"(c.pc & ~255ull) : "memory", "m0"); }
  tok = ld_async(c.ws + WS_F1 + sg * 2048, m * 128);
  STAGE(2, 2);
  if (c.kpos == 2) { KTILE(0); WAITV(15); } else WAITV(11);
  asm volatile("s_waitcnt lgkmcnt(0)" :: "s"(junk), "s"(c.junk) : "memory");
  PIN(tok); BARRIER();
  tok += TOKK;
  if (l == 3) {
    st_one(c.ws + WS_R + sg * 2048, m * 128 + me * 8, tok, 0xffffull);
    *(LAS u64*)(sm + MISC + m * 8) = tok; *(LAS u32*)(sm + MISC + 128) = 0u;
    asm volatile("s_waitcnt lgkmcnt(0)" ::: "memory");
  }
  int slot = 0, kt = 1;
#pragma unroll 1
  for (int st = 3; st < 9; ++st) {
    STAGE(st, slot);
    if (slot == c.kpos) { KTILE(kt); ++kt; WAITV(15); } else WAITV(11);
    slot = slot == 2 ? 0 : slot + 1;
    BARRIER();
  }
  return tok;
}

struct H2 {
  rsrc_t ro, rv;
  u32 vo, vv, ve, oso, eso, vs[3], base;
  int hown, h0, kvsh;
  template <bool EXT> DI void p2(int st) {
    const int h = h0 + ((hown + (st >> 1)) & 7);
    const u32 row = (u32)(h * 128 + (st & 1) * 64) * 1152u, vh = (u32)(h >> kvsh) * (48 * 256) + (st & 1) * 128, d = base + (st & 3) * RP;
#pragma unroll
    for (int i = 0; i < 3; ++i) dma_asm(rv, d + 16384 + i * 4096, vv, vs[i] + vh);
    dma_run<4, 16 * 1152>(ro, d, vo, oso + row);
    if (EXT) dma_asm(ro, d + 28672, ve, eso + row);
  }
};

DI void helper2(Ctx& c, LAS char* sm, int l, u64 tok) {
  const int lane = lane_id(), hkv = 32 >> c.kvsh, l4 = lane >> 4;
  H2 z;
  z.ro = mk(c.wo, 4096 * 1152); z.rv = mk(c.v, (u32)(16 * hkv) * (48 * 256));
  z.vo = l4 * 1152 + ((lane & 15) ^ ((l4 | ((l & 2) << 1)) << 1)) * 16;
  z.vv = (lane >> 3) * 256 + ((lane & 7) ^ (l4 | ((l & 1) << 2))) * 16;
  z.ve = (lane >> 2) * 1152 + ((lane & 3) ^ ((lane >> 5) << 1)) * 16;
  z.oso = c.j * 256 + l * (4 * 1152); z.eso = 1024 + (c.j & 1) * 64 + l * (16 * 1152);
  { const u32 b0 = (u32)(2 * c.bp * hkv) * (48 * 256), b1 = b0 + (u32)hkv * (48 * 256);
    z.vs[0] = b0 + l * 2048; z.vs[1] = l < 2 ? b0 + (l + 4) * 2048 : b1 + (l - 2) * 2048; z.vs[2] = b1 + (l + 2) * 2048; }
  z.base = (u32)(uintptr_t)sm + l * 1024; z.hown = 2 * c.hp; z.h0 = c.h0; z.kvsh = c.kvsh;
  const int sg = c.br * 8 + c.bp, me = c.rank * 4 + c.j;
  const char* F1 = c.ws + WS_F1 + sg * 2048;
  z.p2<true>(0); WAITV(8); BARRIER();
  z.p2<true>(1); WAITV(8); BARRIER();
  const u64 mytok = rdlane64(tok, me);
  const rsrc_t rp = mk(c.ws + WS_P, 256u * 18432u);
  const u32 p4 = 16384 + (l & 1) * 1024;
  LAS char* pl = sm + P_A + l * 1024 + lane * 16;
  LAS char* pl4 = sm + P_A + p4 + lane * 16;
  LAS char* pm = sm + 28672 + l * 1024 + lane * 16;
  LAS char* pm4 = sm + P_B2 + (l & 1) * 1024 + lane * 16;
  const int g = lane >> 4, m = lane & 15, hl = c.rank & 1;
  const bool xown = (c.j >> 1) == hl;
  const u32 cu = blockIdx.x, orow = (u32)((2 * c.bp + (c.rank >> 1)) * 48 + m) * 2304u + c.br * 1152;
  const bool need = m != me && (l < 3 ? (m & 3) == c.j : ((m ^ c.j) & 1) == 0);
  {
    u64 r = ld_async(c.ws + WS_R + sg * 2048, me * 128 + m * 8);
    z.p2<true>(2); z.p2<true>(3); WAITV(16); PIN(r);
    if (!__all(r == mytok))
      for (u32 spins = 0; spins < SPIN_CAP; ++spins) if (__all(ld_now(c.ws + WS_R + sg * 2048, me * 128 + m * 8) == mytok)) break;
  }
  BARRIER();
  z.p2<true>(4); WAITV(16); BARRIER();
  if (XB == 11 && l == 3) st_one(F1, me * 128, mytok, 1ull);
  z.p2<true>(5); WAITV(16); BARRIER();
  if (XB == 12 && l == 3) st_one(F1, me * 128, mytok, 1ull);
  STAMP(40);
  z.p2<true>(6); WAITV(16); BARRIER();
#define PLOAD(pv, t, i) asm volatile("buffer_load_dwordx4 %0, %1, %2, %3 offen sc1" : "=v"(pv[i]) : "v"(lane * 16), "s"(rp), \
    "s"((u32)((c.bp * 4 + ((c.j + (t)) & 3)) * 8 + c.rank * 2 + c.br) * 18432u + ((i) < 4 ? l * 1024 + (i) * 4096 : p4)) : "memory")
#define PUTA(pv, i) do { PIN(pv[i]); if (PPRIO) __builtin_amdgcn_s_setprio(3); if ((i) < 4 || l < 2) *(LAS u32x4*)((i) < 4 ? pl + (i) * 4096 : pl4) = pv[i]; if (PPRIO) __builtin_amdgcn_s_setprio(0); } while (0)
#define PUTB(pv, i) do { PIN(pv[i]); if (PPRIO) __builtin_amdgcn_s_setprio(3); if ((i) < 4 || l < 2) *(LAS u32x4*)((i) < 2 ? pm + (i) * RP : (i) < 4 ? pm + 2 * RP + ((i) - 2) * RP : pm4) = pv[i]; if (PPRIO) __builtin_amdgcn_s_setprio(0); } while (0)
  u32x4 pa[5], pb[5], pc[5];
  {
    const u64 want = __shfl(tok, c.rank * 4 + (lane & 3));
    u64 r = ld_async(F1, (c.rank * 4 + (lane & 3)) * 128);
    z.p2<true>(7); WAITV(8); PIN(r);
    if (!__all(r == want))
      for (u32 spins = 0; spins < SPIN_CAP; ++spins) if (__all(ld_now(F1, (c.rank * 4 + (lane & 3)) * 128) == want)) break;
  }
  STAMP(41);
  PLOAD(pa, 1, 0); PLOAD(pa, 1, 1); PLOAD(pa, 1, 2); PLOAD(pa, 1, 3); PLOAD(pa, 1, 4); WAITV(13); BARRIER();
  z.p2<false>(8); WAITV(7); PUTA(pa, 0); BARRIER();
  PUTA(pa, 1); PUTA(pa, 2); z.p2<false>(9); PLOAD(pb, 2, 0); PLOAD(pb, 2, 1); BARRIER();
  PUTA(pa, 3); PUTA(pa, 4); z.p2<false>(10); PLOAD(pb, 2, 2); WAITV(17); LBARRIER(); STAMP(42);
  PLOAD(pb, 2, 3); PLOAD(pb, 2, 4); z.p2<false>(11); WAITV(7);
  PUTB(pb, 0); PUTB(pb, 1); PUTB(pb, 2); PUTB(pb, 3); PUTB(pb, 4); BARRIER(); STAMP(44);
  z.p2<false>(12); PLOAD(pc, 3, 0); PLOAD(pc, 3, 1); BARRIER();
  z.p2<false>(13); PLOAD(pc, 3, 2); WAITV(17); BARRIER();
  if (l == 3) st_one(c.ws + WS_F3 + sg * 2048, me * 128 + 8, mytok, 1ull);
  z.p2<false>(14); PLOAD(pc, 3, 3); PLOAD(pc, 3, 4); WAITV(19); LBARRIER();
  z.p2<false>(15); WAITV(7);
  PUTA(pc, 0); PUTA(pc, 1); PUTA(pc, 2); PUTA(pc, 3); PUTA(pc, 4); WAITV(0); LBARRIER(); STAMP(46);
#pragma unroll 1
  for (int pass = NODRY; pass < 2; ++pass) {
    const u32 on = pass ? ~0u : 0u;
    const rsrc_t ro = mk(c.out, (768 * 2304) & on);
    if (pass) {
      if (SENT && (l < 3 || xown))
        for (u32 spins = 0; spins < SPIN_CAP; ++spins) if (__all(!need || ld_now(c.ws + WS_F3 + sg * 2048, m * 128 + 8) == tok)) break;
      if (!SENT || l == 3) BARRIER();
      if (l == 3 && !xown) break;
      if (!SENT)
        for (u32 spins = 0; spins < SPIN_CAP; ++spins) if (__all(!need || ld_now(c.ws + WS_F3 + sg * 2048, m * 128 + (l == 3 ? 8 : 0)) == tok)) break;
    }
    if (l < 3) {
      const rsrc_t rm = mk(c.ws + WS_M, (1024u * 6144u) & on);
      u32x4 in[3][2];
      if (SENT && pass) {
        BARRIER();
        __builtin_amdgcn_s_sleep(SDELAY);
      }
      u32 todo = 7;
      for (u32 spins = 0; todo && spins < SPIN_CAP; ++spins) {
#pragma unroll
        for (int r = 0; r < 3; ++r) {
          const u32 pcu = (cu & ~6u) | (((c.rank + 1 + r) & 3) << 1);
          if ((todo >> r) & 1) {
#pragma unroll
            for (int i = 0; i < 2; ++i) in[r][i] = __builtin_amdgcn_raw_buffer_load_b128(rm, lane * 16 + i * 1024, (pcu * 4 + c.rank) * 6144u + l * 2048, 16);
          }
        }
        if (!SENT) break;
        todo = 0;
#pragma unroll
        for (int r = 0; r < 3; ++r) {
          const u32 mx = max(max(max(in[r][0][0], in[r][0][1]), max(in[r][0][2], in[r][0][3])), max(max(in[r][1][0], in[r][1][1]), max(in[r][1][2], in[r][1][3])));
          if (__any(mx == ~0u)) todo |= 1u << r;
        }
        asm volatile("" ::: "memory");
      }
      STAMP(29);
#pragma unroll
      for (int c2 = 0; c2 < 2; ++c2) {
        f32x4 a = *(LAS f32x4*)(sm + OWN + (l * 4 + 2 * c2) * 1024 + lane * 16), d = *(LAS f32x4*)(sm + OWN + (l * 4 + 2 * c2 + 1) * 1024 + lane * 16);
#pragma unroll
        for (int r = 0; r < 3; ++r) {
          const u32x4 v = in[r][c2];
          a += (f32x4){bflo(v[0]), bfhi(v[0]), bflo(v[1]), bfhi(v[1])};
          d += (f32x4){bflo(v[2]), bfhi(v[2]), bflo(v[3]), bfhi(v[3])};
        }
        const u32x2 pa = pk4(a), pd = pk4(d);
        auto s0 = __builtin_amdgcn_permlane16_swap(pa[0], pd[0], false, false);
        auto s1 = __builtin_amdgcn_permlane16_swap(pa[1], pd[1], false, false);
        __builtin_amdgcn_raw_buffer_store_b128((u32x4){s0[0], s1[0], s0[1], s1[1]}, ro,
            orow + l * (16 * 2304) + (128 * c.j + 64 * hl + 32 * c2 + 16 * (g & 1) + 8 * (g >> 1)) * 2, 0, 0);
      }
    } else {
      const rsrc_t rxx = mk(c.ws + WS_X, (1024u * 2048u) & on);
      u32x2 xin[7][3];
#pragma unroll
      for (int r = 0; r < 7; ++r) {
        const u32 pcu = ((cu & ~6u) | (((c.rank + 1 + r) & 3) << 1)) ^ (r >= 3 ? 16u : 0u);
#pragma unroll
        for (int n = 0; n < 3; ++n) xin[r][n] = __builtin_amdgcn_raw_buffer_load_b64(rxx, lane * 8 + n * 512, (pcu * 4 + c.rank) * 2048u, 16);
      }
#pragma unroll
      for (int n = 0; n < 3; ++n) {
        f32x4 a = *(LAS f32x4*)(sm + XL + n * 1024 + lane * 16);
#pragma unroll
        for (int r = 0; r < 7; ++r) a += (f32x4){bflo(xin[r][n][0]), bfhi(xin[r][n][0]), bflo(xin[r][n][1]), bfhi(xin[r][n][1])};
        __builtin_amdgcn_raw_buffer_store_b64(pk4(a), ro, orow + n * (16 * 2304) + (512 + 32 * (c.j & 1) + 16 * hl + 4 * g) * 2, 0, 0);
      }
    }
  }
  STAMP(32);
}

template <bool ADV, int I0, int I1> DI void half1(f32x4 (&Q)[8][3], const u32x4 (&A)[8], const u32x4 (&B)[3], u32x4 (&An)[8], u32x4 (&Bn)[3],
                                  LAS char* (&w)[8], LAS char* (&x)[2], int u, int adv) {
  u32x2 lo[8];
#pragma unroll
  for (int i = I0; i < I1; ++i) {
    Q[i / 3][i % 3] = mfma(A[i / 3], B[i % 3], Q[i / 3][i % 3]);
    constexpr int what[19] = {70, 71, 00, 01, 100, 10, 11, 101, 20, 21, 102, 30, 31, 60, 40, 41, 61, 50, 51};
    if (i < 19) {
      const int k = what[i];
      if (k >= 100) Bn[k - 100] = lds128(x[u] + (k - 100) * 2048);
      else if (k % 10 == 0) lo[k / 10] = trd(w[k / 10] + u * 8192);
      else An[k / 10] = cat(lo[k / 10], trd(w[k / 10] + u * 8192 + 1024));
    } else if (ADV) {
      if (i < 23) { w[2 * (i - 19)] = vadd(w[2 * (i - 19)], adv); w[2 * (i - 19) + 1] = vadd(w[2 * (i - 19) + 1], adv); }
      else { x[0] = vadd(x[0], adv); x[1] = vadd(x[1], adv); }
    }
    SB();
  }
}
struct G2 { u32x4 v[3], w[5]; u32x2 lo; };
struct A2 { LAS char* w[4]; LAS char* e; LAS char* v[2]; };
DI void shift(A2& a, int d) {
#pragma unroll
  for (int i = 0; i < 4; ++i) a.w[i] = vadd(a.w[i], d);
  a.e = vadd(a.e, d); a.v[0] = vadd(a.v[0], d); a.v[1] = vadd(a.v[1], d);
}
template <bool EXT> DI void rd2(int i, G2& f, const A2& a, int off, int u) {
  if (i < 3) f.v[i] = lds128(a.v[u] + off + 16384 + i * 2048);
  else if (i < 11) { const int ct = (i - 3) >> 1; if ((i - 3) & 1) f.w[ct] = cat(f.lo, trd(a.w[ct] + off + u * 8192 + 1024)); else f.lo = trd(a.w[ct] + off + u * 8192); }
  else if (EXT && i == 11) f.lo = trd(a.e + off + 28672 + u * 2048);
  else if (EXT && i == 12) f.w[4] = cat(f.lo, trd(a.e + off + 28672 + u * 2048 + 256));
}
template <bool E, bool FIRST, bool READ, bool PACK, int DBL, int SH = 0>
DI void step2(f32x4 (&U)[3][5], const G2& f, G2& fn, A2& a, int off, int u, u32x4 (&UA)[5], u32x2 (&UC)[5]) {
  const f32x4 z = {0.f, 0.f, 0.f, 0.f};
#pragma unroll
  for (int i = 0; i < (E ? 15 : 12); ++i) {
    const int ct = i / 3, kt = i % 3;
    if (PACK) { const u32x2 t = pk4(U[kt][ct]); if (kt == 0) { UA[ct][0] = t[0]; UA[ct][1] = t[1]; } else if (kt == 1) { UA[ct][2] = t[0]; UA[ct][3] = t[1]; } else UC[ct] = t; SB(); }
    U[kt][ct] = mfma(f.v[kt], f.w[ct], FIRST ? z : U[kt][ct]);
    if (READ) { if (i < DBL) { rd2<E>(2 * i, fn, a, off, u); rd2<E>(2 * i + 1, fn, a, off, u); } else rd2<E>(i + DBL, fn, a, off, u); }
    if (SH) { const int k = i - (E ? 11 : 8);
      if (k == 0) { a.v[0] = vadd(a.v[0], SH); a.v[1] = vadd(a.v[1], SH); }
      if (k == 1) { a.w[0] = vadd(a.w[0], SH); a.w[1] = vadd(a.w[1], SH); }
      if (k == 2) { a.w[2] = vadd(a.w[2], SH); a.w[3] = vadd(a.w[3], SH); }
      if (k == 3) a.e = vadd(a.e, SH); }
    SB();
  }
}
template <bool PE, bool CE, bool READ, bool FIRST, int BS>
DI void finish2(f32x4 (&O)[5][3], f32x4 (&U)[3][5], const u32x4 (&A0)[5], const u32x2 (&C0)[5], G2& f0, G2& f1, const A2& a, int off, LAS char* p, LAS char* pc, LAS char* pc2) {
  constexpr int N = PE ? 15 : 12;
  u32x4 pa0[3], pa1[3], pcc[3], A1[5]; u32x2 C1[5], lo[3];
#pragma unroll
  for (int i = 0; i < N; ++i) {
    U[i % 3][i / 3] = mfma(f1.v[i % 3], f1.w[i / 3], U[i % 3][i / 3]);
    if (i < 3) pa0[i] = lds128(p + i * 1024);
    else if (i < 6) pa1[i - 3] = lds128(p + BS + (i - 3) * 1024);
    else if (READ) rd2<CE>(i - 6, f0, a, off, 0);
    SB();
  }
#pragma unroll
  for (int i = 0; i < N; ++i) {
    const int ct = i / 3, n = i % 3;
    O[ct][n] = mfma(A0[ct], pa0[n], FIRST ? (f32x4){0.f, 0.f, 0.f, 0.f} : O[ct][n]);
    const u32x2 t = pk4(U[n][ct]);
    if (n == 0) { A1[ct][0] = t[0]; A1[ct][1] = t[1]; } else if (n == 1) { A1[ct][2] = t[0]; A1[ct][3] = t[1]; } else C1[ct] = t;
    SB();
  }
#pragma unroll
  for (int i = 0; i < N; ++i) {
    O[i / 3][i % 3] = mfma(A1[i / 3], pa1[i % 3], O[i / 3][i % 3]);
    if (i < 3) lo[i] = lds64(i < 2 ? pc + i * 512 : pc2);
    else if (i < 6) pcc[i - 3] = cat(lo[i - 3], lds64(i < 5 ? pc + BS + (i - 3) * 512 : pc2 + 512));
    else if (READ) rd2<CE>(i + N - 12, f0, a, off, 0);
    SB();
  }
  if (READ && N == 12) rd2<CE>(12, f0, a, off, 0);
#pragma unroll
  for (int i = 0; i < N; ++i) {
    O[i / 3][i % 3] = mfma(cat(C0[i / 3], C1[i / 3]), pcc[i % 3], O[i / 3][i % 3]);
    if (READ) rd2<CE>(i, f1, a, off, 1);
    SB();
  }
  if (READ && N == 12) rd2<CE>(12, f1, a, off, 1);
}
DI float max3(float a, float b, float c) { float r; asm("v_max3_f32 %0, %1, %2, %3" : "=v"(r) : "v"(a), "v"(b), "v"(c)); return r; }
DI float max2(float a, float b) { float r; asm("v_max_f32 %0, %1, %2" : "=v"(r) : "v"(a), "v"(b)); return r; }

DI void tail(Ctx& c, LAS char* sm, int wv, f32x4 (&O)[5][3]) {
  const int lane = lane_id();
  if (wv == c.rank) {
#pragma unroll
    for (int n = 0; n < 3; ++n)
#pragma unroll
      for (int ct = 0; ct < 4; ++ct) *(LAS f32x4*)(sm + OWN + (n * 4 + ct) * 1024 + lane * 16) = O[ct][n];
    LBARRIER();
    return;
  }
  const rsrc_t rm = mk(c.ws + WS_M, 1024u * 6144u);
#pragma unroll
  for (int n = 0; n < 3; ++n)
#pragma unroll
    for (int c2 = 0; c2 < 2; ++c2) __builtin_amdgcn_raw_buffer_store_b128(cat(pk4(O[2 * c2][n]), pk4(O[2 * c2 + 1][n])), rm, (blockIdx.x * 4 + wv) * 6144u + lane * 16 + (n * 2 + c2) * 1024, 0, 16);
  const int me = c.rank * 4 + c.j;
  const u64 tok = *(LAS u64*)(sm + MISC + me * 8);
  BARRIER();
  WAITV(0);
  STAMP(28);
  u32 old;
  asm volatile("s_mov_b64 exec, 1\n\tds_add_rtn_u32 %0, %1, %2\n\ts_waitcnt lgkmcnt(0)\n\ts_mov_b64 exec, -1" : "=&v"(old) : "v"((u32)(uintptr_t)sm + MISC + 128), "v"(1u) : "memory");
  if (__builtin_amdgcn_readfirstlane(old) == 2u) st_one(c.ws + WS_F3 + (c.br * 8 + c.bp) * 2048, me * 128, tok, 1ull);
}

DI void compute(Ctx& c, LAS char* sm, int wv) {
  char* ws = c.ws;
  if (CPRIO) __builtin_amdgcn_s_setprio(CPRIO);
  const int bl = wv >> 1, hl = wv & 1;
  const u32 cu = blockIdx.x;
  int lane = lane_id(), g = lane >> 4, m = lane & 15, q = m >> 2, p = lane & 3;
  u32 wbase = (8 * g + q) * 256 + 32 * (q | ((g & 1) << 2)) + 8 * p;
  u32 xbase = (bl * 48 + m) * 128 + ((g ^ (m >> 1)) * 16);
  u32x4 Kf[3][4];
  f32x4 Q[8][3];
  u32x4 A1[8], B1[3];
  LAS char* aw[8]; LAS char* ax[2];
#pragma unroll
  for (int T = 0; T < 8; ++T) { A1[T] = (u32x4){0, 0, 0, 0}; PIN(A1[T]);
    aw[T] = vadd(sm + (wbase ^ (T << 5)), 12288 + hl * 16384);
#pragma unroll
    for (int n = 0; n < 3; ++n) { Q[T][n] = (f32x4){0.f, 0.f, 0.f, 0.f}; PIN(Q[T][n]); } }
#pragma unroll
  for (int n = 0; n < 3; ++n) { B1[n] = (u32x4){0, 0, 0, 0}; PIN(B1[n]); }
  ax[0] = vadd(sm + xbase, 0); ax[1] = vadd(sm + (xbase ^ 64), 0);
  SB();
  STAMP(33); asm volatile("s_waitcnt vmcnt(0) lgkmcnt(0)" :: "s"(c.junk) : "memory"); STAMP(34); BARRIER();
  STAMP(0);
#pragma unroll
  for (int kt = 0; kt < 3; ++kt) {
#pragma unroll 1
    for (int s = 0; s < 3; ++s) {
      u32x4 A0[8], B0[3];
      half1<false, 0, 20>(Q, A1, B1, A0, B0, aw, ax, 0, 0);
      if (s == c.kpos) {
        const u32 l = lane_id(), kb = KS + wv * 4096 + (l & 15) * 256 + ((l >> 5) ^ (l & 15)) * 16 + ((l >> 1) & 8);
#pragma unroll
        for (int i = 0; i < 4; ++i) Kf[kt][i] = cat(lds64(sm + (kb ^ (i << 6))), lds64(sm + (kb ^ (i << 6) ^ 32)));
      }
      half1<false, 20, 24>(Q, A1, B1, A0, B0, aw, ax, 0, 0);
      half1<true, 0, 24>(Q, A0, B0, A1, B1, aw, ax, 1, s == 2 ? -2 * QP : QP);
      LBARRIER();
      STAMP(1 + 3 * kt + s);
    }
  }
  SB();
  lane = lane_id(); g = lane >> 4; m = lane & 15; q = m >> 2; p = lane & 3;
  wbase = (8 * g + q) * 256 + 32 * (q | ((g & 1) << 2)) + 8 * p;
  xbase = (bl * 48 + m) * 128 + ((g ^ (m >> 1)) * 16);
  A2 a;
#pragma unroll
  for (int ct = 0; ct < 4; ++ct) a.w[ct] = sm + (wbase ^ ((4 * hl + ct) << 5));
  a.e = sm + (8 * g + q) * 64 + 32 * (hl ^ (g & 1)) + 8 * p;
  a.v[0] = sm + xbase; a.v[1] = sm + (xbase ^ 64);
  G2 f0, f1;
  f32x4 O[5][3], U[3][5];
  {
    u32x4 qf[3][4];
    f32x4 S[3][3];
#pragma unroll
    for (int i = 0; i < 24; ++i) {
      Q[i & 7][i >> 3] = mfma(A1[i & 7], B1[i >> 3], Q[i & 7][i >> 3]);
      if (i >= 8) { const u32x2 t = pk4(Q[i & 7][(i >> 3) - 1]); qf[(i >> 3) - 1][(i & 7) >> 1][2 * (i & 1)] = t[0]; qf[(i >> 3) - 1][(i & 7) >> 1][2 * (i & 1) + 1] = t[1]; }
      SB();
    }
    STAMP(36);
#pragma unroll
    for (int i = 0; i < 36; ++i) {
      const int n = i / 12, s = (i % 12) / 3, kt = i % 3;
      S[n][kt] = mfma(Kf[kt][s], qf[n][s], s == 0 ? (f32x4){0.f, 0.f, 0.f, 0.f} : S[n][kt]);
      if (i >= 2 && i < 10) { const u32x2 t = pk4(Q[i - 2][2]); qf[2][(i - 2) >> 1][2 * (i & 1)] = t[0]; qf[2][(i - 2) >> 1][2 * (i & 1) + 1] = t[1]; }
      if (i >= 10 && i < 23) rd2<true>(i - 10, f0, a, 0, 0);
      if (i >= 23) rd2<true>(i - 23, f1, a, 0, 1);
      SB();
    }
    STAMP(37);
    const rsrc_t rp = mk(ws + WS_P, 256u * 18432u);
    const u32 pso = cu * 18432u + wv * 4096;
    LAS char* pl = vadd(sm + lane * 16, P_A + wv * 4096);
    LAS char* pl2 = vadd(sm + lane * 8, P_A + wv * 4096 + 3072);
    const int c2o = 16384 - 3072 - wv * 3584;
    float mx[3], sum[3];
    int um = 0;
#define UM() do { if (um < 15) { U[um % 3][um / 3] = mfma(f0.v[um % 3], f0.w[um / 3], (f32x4){0.f, 0.f, 0.f, 0.f}); ++um; } SB(); } while (0)
#pragma unroll
    for (int n = 0; n < 3; ++n)
      mx[n] = fmaxf(fmaxf(fmaxf(fmaxf(fmaxf(fmaxf(S[n][0][0], S[n][0][1]), S[n][0][2]), S[n][0][3]), S[n][1][0]), fmaxf(fmaxf(S[n][1][1], S[n][1][2]), S[n][1][3])),
                    fmaxf(fmaxf(fmaxf(S[n][2][0], S[n][2][1]), S[n][2][2]), S[n][2][3]));
#pragma unroll
    for (int n = 0; n < 3; ++n) { auto r = __builtin_amdgcn_permlane16_swap(__float_as_uint(mx[n]), __float_as_uint(mx[n]), false, false); mx[n] = fmaxf(__uint_as_float(r[0]), __uint_as_float(r[1])); }
#pragma unroll
    for (int n = 0; n < 3; ++n) { auto r = __builtin_amdgcn_permlane32_swap(__float_as_uint(mx[n]), __float_as_uint(mx[n]), false, false); mx[n] = fmaxf(__uint_as_float(r[0]), __uint_as_float(r[1])) * -0.12751743344863963f; }
    SB();
#pragma unroll
    for (int n = 0; n < 3; ++n)
#pragma unroll
      for (int kt = 0; kt < 3; ++kt) {
#pragma unroll
        for (int i = 0; i < 4; ++i) S[n][kt][i] = __builtin_amdgcn_exp2f(__builtin_fmaf(S[n][kt][i], 0.12751743344863963f, mx[n]));
        UM();
      }
    STAMP(38);
#pragma unroll
    for (int n = 0; n < 3; ++n) { const f32x4 s4 = S[n][0] + S[n][1] + S[n][2]; sum[n] = (s4[0] + s4[1]) + (s4[2] + s4[3]); UM(); }
#pragma unroll
    for (int n = 0; n < 3; ++n) { auto r = __builtin_amdgcn_permlane16_swap(__float_as_uint(sum[n]), __float_as_uint(sum[n]), false, false); sum[n] = __uint_as_float(r[0]) + __uint_as_float(r[1]); }
#pragma unroll
    for (int n = 0; n < 3; ++n) { auto r = __builtin_amdgcn_permlane32_swap(__float_as_uint(sum[n]), __float_as_uint(sum[n]), false, false); sum[n] = __builtin_amdgcn_rcpf(__uint_as_float(r[0]) + __uint_as_float(r[1])); }
    SB();
    STAMP(39);
#pragma unroll
    for (int n = 0; n < 3; ++n) {
      const u32x4 pa = cat(pk4(S[n][0] * sum[n]), pk4(S[n][1] * sum[n]));
      const u32x2 pc = pk4(S[n][2] * sum[n]);
      __builtin_amdgcn_raw_buffer_store_b128(pa, rp, pso + lane * 16 + n * 1024, 0, 16);
      __builtin_amdgcn_raw_buffer_store_b64(pc, rp, lane * 8 + 3072 + (n < 2 ? n * 512 : 0), pso + (n < 2 ? 0 : c2o), 16);
      *(LAS u32x4*)(pl + n * 1024) = pa;
      *(LAS u32x2*)(n < 2 ? pl2 + n * 512 : vadd(pl2, c2o)) = pc;
      UM();
    }
  }
  STAMP(26);
  SB();
  u32x4 UA[5]; u32x2 UC[5];
  const bool xown = (wv == c.rank) && ((c.j >> 1) == (c.rank & 1));
#define ENDI(gi) do { if ((gi) == 19 || (gi) + 1 == XB) WAITV(0); if ((gi) < 22) LBARRIER(); STAMP(1 + (gi)); SB(); } while (0)
#define SLOT(gi) ((((gi) - 9) & 1) * RP)
  LAS char *pp0, *pp1, *pp2;
#define PPTR(INB) do { const int l_ = lane_id(), pb_ = (INB) ? 28672 + bl * 2 * RP : P_A + bl * 8192; \
    pp0 = vadd(sm + l_ * 16, pb_); pp1 = vadd(sm + l_ * 8, pb_ + 3072); pp2 = vadd(sm + l_ * 8, ((INB) ? P_B2 : P_A + 16384) + bl * 1024); SB(); } while (0)
#define FIN(PE, CE, READ, FIRST, INB, off) finish2<PE, CE, READ, FIRST, (INB) ? RP : 4096>(O, U, UA, UC, f0, f1, a, off, pp0, pp1, pp2)
#define BODY(t, PE, CE) do { \
    constexpr int g0 = 9 + 4 * (t); \
    if ((t) > 0) { \
      FIN(PE, CE, true, (t) == 1, (t) == 3, SLOT(g0)); \
      if ((t) == 2) { \
        if (xown) { _Pragma("unroll") for (int n = 0; n < 3; ++n) *(LAS f32x4*)(sm + XL + n * 1024 + lane_id() * 16) = O[4][n]; } \
        else { const rsrc_t rxx = mk(ws + WS_X, 1024u * 2048u); \
          _Pragma("unroll") for (int n = 0; n < 3; ++n) __builtin_amdgcn_raw_buffer_store_b64(pk4(O[4][n]), rxx, lane_id() * 8 + n * 512, (cu * 4 + wv) * 2048u, 16); } \
        if (SENT && wv != c.rank) { const rsrc_t rm = mk(ws + WS_M, 1024u * 6144u); const u32 ones = ~0u; \
          _Pragma("unroll") for (int i = 0; i < 6; ++i) __builtin_amdgcn_raw_buffer_store_b128((u32x4){ones, ones, ones, ones}, rm, lane_id() * 16 + (i & 3) * 1024, (cu * 4 + wv) * 6144u + (i >> 2) * 4096, 16); } } \
      step2<CE, true, false, false, 0>(U, f0, f1, a, 0, 0, UA, UC); } \
    ENDI(g0); \
    step2<CE, false, true, false, 0>(U, f1, f0, a, SLOT(g0 + 1), 0, UA, UC); \
    step2<CE, false, true, false, 3, 2 * RP>(U, f0, f1, a, SLOT(g0 + 1), 1, UA, UC); ENDI(g0 + 1); \
    step2<CE, false, true, false, 0>(U, f1, f0, a, SLOT(g0 + 2), 0, UA, UC); \
    step2<CE, true, true, true, 3>(U, f0, f1, a, SLOT(g0 + 2), 1, UA, UC); ENDI(g0 + 2); \
    step2<CE, false, true, false, 0>(U, f1, f0, a, SLOT(g0 + 3), 0, UA, UC); \
    step2<CE, false, true, false, 3, -2 * RP>(U, f0, f1, a, SLOT(g0 + 3), 1, UA, UC); PPTR((t) == 2); ENDI(g0 + 3); \
  } while (0)
  BODY(0, true, true);
  BODY(1, true, true);
  BODY(2, true, false);
  BODY(3, false, false);
  FIN(false, false, false, false, false, 0);
  STAMP(27);
  tail(c, sm, wv, O);
}

__global__ __launch_bounds__(512, 2) void fused_attn_block(const char* xA, const char* kA, const char* vA, const char* wqA, const char* woA,
                                                          const char* xB, const char* kB, const char* vB, const char* wqB, const char* woB,
                                                          char* out, char* ws) {
  __shared__ __attribute__((aligned(256))) char smem[LDS_BYTES];
  LAS char* sm = (LAS char*)smem;
  const int wv = __builtin_amdgcn_readfirstlane(threadIdx.x >> 6);
  Ctx c;
  c.stamps = 0; c.ta = 0; c.tb = 0;
  STAMP(63);
  const int id = blockIdx.x;
  c.br = id & 1; c.rank = (id >> 1) & 3; c.j = (id >> 3) & 3; c.bp = id >> 5;
  c.h0 = c.rank * 8; c.kvsh = 1 - c.br;
  c.hp = ROTH ? (c.j + c.bp) & 3 : c.j;
  { const int g3 = (c.bp + c.hp) % 3;
    c.ord = g3 == 0 ? 0x875462130ull : g3 == 1 ? 0x865732401ull : 0x768435102ull;
    c.kpos = KPOS ? KPOS : (g3 == 1 ? 2 : 1); }
  c.rot = ROTK == 4 ? (int)(c.ord & 15) : ROTK == 1 ? (c.bp + c.hp) % 3 : ROTK == 3 ? ((c.bp + c.hp) % 3) * 3 : ROTK == 2 ? ((c.bp + c.hp) & 1) * 4 : 0;
  {
    const u32 lane = lane_id(), l4 = lane >> 4, k0 = (u32)c.ord & 15u;
    u32 vx = (lane >> 3) * 1152 + ((lane & 7) ^ (l4 | ((wv & 1) << 2))) * 16;
    u32 vq = l4 * 8192 + ((lane & 15) ^ ((l4 | ((wv & 2) << 1)) << 1)) * 16;
    u32 xs = (u32)(c.bp * 96 + wv * 8) * 1152u + k0 * 128, qs = (u32)(c.h0 + 2 * c.hp) * 256u + wv * (4 * 8192) + k0 * (64 * 8192);
    u32 d = (u32)(uintptr_t)sm + wv * 1024;
    u32 d5 = wv < 4 ? d + 8192 : (u32)(uintptr_t)sm + JUNK, x5 = wv < 4 ? xs + 64 * 1152 : 0x7f000000u;
    asm volatile("s_getpc_b64 %0" : "=s"(c.pc));
    asm volatile("" : "+v"(vx), "+v"(vq), "+s"(xs), "+s"(qs), "+s"(d), "+s"(d5), "+s"(x5));
    SB();
    c.x = c.br ? xB : xA; c.k = c.br ? kB : kA; c.v = c.br ? vB : vA; c.wq = c.br ? wqB : wqA; c.wo = c.br ? woB : woA;
    c.out = out; c.ws = ws;
    u32 junk = 0;
    if (STLB) asm volatile("s_load_dword %0, %1, %2" : "+s"(junk) : "s"(c.wq), "s"(qs) : "memory");
    const rsrc_t rx = mk(c.x, 768 * 1152), rq = mk(c.wq, 576 * 8192);
    dma_asm(rx, d, vx, xs);
#pragma unroll
    for (int i = 0; i < 4; ++i) dma_asm(rq, d + 12288 + i * 8192, vq, qs + (i & 1) * (32 * 8192) + (i >> 1) * 256);
    dma_asm(rx, d5, vx, x5);
    c.junk = junk;
    STAMP(30);
  }
  u64 tok = 0;
  { int w2 = wv; asm volatile("" : "+s"(w2));
    if (__builtin_expect(w2 >= 4, 1)) tok = helper1(c, sm, w2 - 4); }
  if (__builtin_expect(wv < 4, 1)) {
    if (ABL_NOCOMP) { STAMP(33); WAITV(0); STAMP(34); BARRIER(); STAMP(0); for (int g = 1; g < 24; ++g) { BARRIER(); STAMP(g < 23 ? g : g + 2); } }
    else compute(c, sm, wv);
  } else helper2(c, sm, wv - 4, tok);
  STAMP(62);
  if (wv == AMP_W) { const u64 t0 = __builtin_amdgcn_s_memrealtime(); const u64 d = (u64)(c.tb - c.ta) * 20u; while (__builtin_amdgcn_s_memrealtime() - t0 < d) __builtin_amdgcn_s_sleep(4); }
}

extern "C" void kernel_launch(void* const* d_in, const int* in_sizes, int n_in, void* d_out, int out_size, void* d_ws, size_t ws_size,
                              hipStream_t stream) {
  const char* const* in = (const char* const*)d_in;
  fused_attn_block<<<256, 512, 0, stream>>>(in[0], in[1], in[2], in[3], in[4], in[5], in[6], in[7], in[8], in[9], (char*)d_out, (char*)d_ws);
}
```

```cpp
#include <hip/hip_runtime.h>
#include <stdint.h>

typedef unsigned u32;
typedef unsigned long long u64;
typedef u32 u32x4 __attribute__((ext_vector_type(4)));
typedef u32 u32x2 __attribute__((ext_vector_type(2)));
typedef float f32x4 __attribute__((ext_vector_type(4)));
typedef float f32x2 __attribute__((ext_vector_type(2)));
typedef short s16x4 __attribute__((ext_vector_type(4)));
typedef __bf16 bf16x8 __attribute__((ext_vector_type(8)));
typedef __bf16 bf16x2 __attribute__((ext_vector_type(2)));
#define LAS __attribute__((address_space(3)))
#define GAS __attribute__((address_space(1)))
#define DI __device__ __forceinline__
typedef __amdgpu_buffer_rsrc_t rsrc_t;

constexpr int QP = 45056;
constexpr int RP = 32768;
constexpr int KS = 135168;
constexpr int P_A = 131072;
constexpr int XL = 149504;
constexpr int P_B2 = 154880;
constexpr int OWN = 0;
constexpr int MISC = 153600;
constexpr int JUNK = 153856;
constexpr int LDS_BYTES = 156928;
constexpr u32 WS_F1 = 0;
constexpr u32 WS_F3 = 32768;
constexpr u32 WS_R = 65536;
constexpr u32 WS_P = 131072;
constexpr u32 WS_M = WS_P + 256u * 18432u;
constexpr u32 WS_X = WS_M + 1024u * 6144u;
constexpr u64 TOKK = 0x9E3779B97F4A7C15ull;
constexpr u32 SPIN_CAP = 1u << 20;

DI rsrc_t mk(const void* p, u32 bytes) { return __builtin_amdgcn_make_buffer_rsrc((void*)p, 0, bytes, 0x00020000); }
DI void dma_asm(rsrc_t r, u32 dst, u32 voff, u32 soff) {
  asm volatile("s_mov_b32 m0, %0\n\ts_nop 0\n\tbuffer_load_dwordx4 %1, %2, %3 offen lds" :: "s"(dst), "v"(voff), "s"(r), "s"(soff) : "memory", "m0");
}
template <int N, int STEP> DI void dma_run(rsrc_t r, u32 dst, u32 voff, u32 soff) {
  u32 t;
#define PIECE_ "s_add_u32 m0, m0, 0x1000\n\ts_add_u32 %0, %0, %5\n\tbuffer_load_dwordx4 %2, %3, %0 offen lds\n\t"
#define FIRST_ "s_mov_b32 m0, %1\n\ts_mov_b32 %0, %4\n\tbuffer_load_dwordx4 %2, %3, %0 offen lds\n\t"
  if (N == 3) asm volatile(FIRST_ PIECE_ PIECE_ : "=&s"(t) : "s"(dst), "v"(voff), "s"(r), "s"(soff), "n"(STEP) : "memory", "m0", "scc");
  if (N == 4) asm volatile(FIRST_ PIECE_ PIECE_ PIECE_ : "=&s"(t) : "s"(dst), "v"(voff), "s"(r), "s"(soff), "n"(STEP) : "memory", "m0", "scc");
}
#define WAITV(n) asm volatile("s_waitcnt vmcnt(%0)" ::"i"(n) : "memory")
#define BARRIER() asm volatile("s_barrier" ::: "memory")
#define LBARRIER() do { __builtin_amdgcn_s_waitcnt(0xC07F); asm volatile("s_barrier" ::: "memory"); } while (0)
#define SB() __builtin_amdgcn_sched_barrier(0)
#define PIN(x) asm volatile("" : "+v"(x))
DI int lane_id() { int l = __builtin_amdgcn_mbcnt_hi(~0u, __builtin_amdgcn_mbcnt_lo(~0u, 0u)); PIN(l); return l; }
DI f32x4 mfma(u32x4 a, u32x4 b, f32x4 c) { return __builtin_amdgcn_mfma_f32_16x16x32_bf16(__builtin_bit_cast(bf16x8, a), __builtin_bit_cast(bf16x8, b), c, 0, 0, 0); }
DI u32 pk(float a, float b) { f32x2 f = {a, b}; return __builtin_bit_cast(u32, __builtin_convertvector(f, bf16x2)); }
DI u32x2 pk4(f32x4 v) { u32x2 r = {pk(v[0], v[1]), pk(v[2], v[3])}; return r; }
DI u32x4 cat(u32x2 a, u32x2 b) { u32x4 r = {a[0], a[1], b[0], b[1]}; return r; }
DI u32x2 trd(LAS char* p) { return __builtin_bit_cast(u32x2, __builtin_amdgcn_ds_read_tr16_b64_v4i16((LAS s16x4*)p)); }
DI u32x4 lds128(LAS char* p) { return *(LAS u32x4*)p; }
DI u32x2 lds64(LAS char* p) { return *(LAS u32x2*)p; }
DI float bflo(u32 x) { return __builtin_bit_cast(float, x << 16); }
DI float bfhi(u32 x) { return __builtin_bit_cast(float, x & 0xffff0000u); }
DI u64 rdlane64(u64 v, int l) { return ((u64)(u32)__builtin_amdgcn_readlane((int)(v >> 32), l) << 32) | (u32)__builtin_amdgcn_readlane((int)v, l); }
DI LAS char* vadd(LAS char* p, int d) { u32 r; asm volatile("v_add_u32 %0, %1, %2" : "=v"(r) : "s"(d), "v"((u32)(uintptr_t)p)); return (LAS char*)(uintptr_t)r; }

struct Ctx {
  const char *x, *k, *v, *wq, *wo;
  u64 pc;
  char *out, *ws;
  u32 junk;
  u64 ord;
  int br, rank, bp, j;
  int h0, kvsh, hp, kpos;
};

DI u64 ld_async(const char* base, u32 off) { u64 r; asm volatile("global_load_dwordx2 %0, %1, %2 sc1" : "=v"(r) : "v"(off), "s"(base) : "memory"); return r; }
DI u64 ld_now(const char* base, u32 off) { u64 r; asm volatile("global_load_dwordx2 %0, %1, %2 sc1\n\ts_waitcnt vmcnt(0)" : "=&v"(r) : "v"(off), "s"(base) : "memory"); return r; }
DI void st_one(const char* base, u32 off, u64 v, u64 lanes) {
  asm volatile("s_mov_b64 exec, %3\n\tglobal_store_dwordx2 %0, %1, %2 sc1\n\ts_mov_b64 exec, -1" :: "v"(off), "v"(v), "s"(base), "s"(lanes) : "memory");
}

DI u64 helper1(Ctx& c, LAS char* sm, int l) {
  const int lane = lane_id(), hkv = 32 >> c.kvsh;
  const u32 base = (u32)(uintptr_t)sm;
  const rsrc_t rx = mk(c.x, 768 * 1152), rq = mk(c.wq, 576 * 8192), rk = mk(c.k, (u32)(16 * hkv) * (48 * 256));
  const u32 l4 = lane >> 4;
  const u32 vx = (lane >> 3) * 1152 + ((lane & 7) ^ (l4 | ((l & 1) << 2))) * 16;
  const u32 vq = l4 * 8192 + ((lane & 15) ^ ((l4 | ((l & 2) << 1)) << 1)) * 16;
  const u32 vk = l4 * 256 + ((lane & 15) ^ (l4 | (l << 2))) * 16;
  const u32 xs = (u32)(c.bp * 96 + l * 8) * 1152u, qs = (u32)(c.h0 + 2 * c.hp) * 256u + l * (4 * 8192);
  const u32 k0 = (u32)(2 * c.bp * hkv + ((c.h0 + 2 * c.hp) >> c.kvsh)) * (48 * 256) + l * 1024, k1 = c.kvsh ? 0u : 48u * 256u, k2 = (u32)hkv * (48 * 256);
  const int sg = c.br * 8 + c.bp, me = c.rank * 4 + c.j, m = lane & 15;
  u64 tok = 0;

#define STAGE(st_, slot_) do { const u32 k_ = (u32)(c.ord >> (4 * (st_))) & 15u, d_ = base + (slot_) * QP + l * 1024; \
    dma_run<3, 32 * 1152>(rx, d_, vx, xs + k_ * 128); \
    dma_run<4, 16 * 8192>(rq, d_ + 12288, vq, qs + k_ * (64 * 8192)); \
    dma_run<4, 16 * 8192>(rq, d_ + 28672, vq, qs + k_ * (64 * 8192) + 256); } while (0)
#define KTILE(kt_) do { _Pragma("unroll") for (int i = 0; i < 4; ++i) dma_asm(rk, base + KS + l * 1024 + i * 4096, vk, k0 + (i & 1) * k1 + (i >> 1) * k2 + (kt_) * 4096); } while (0)
  u32 junk = c.junk;
  {
    const u32 o0 = ((2 * c.bp + (l & 1)) * hkv + (c.h0 >> c.kvsh)) * (48 * 256), o1 = (c.h0 * 128 + l * 341) * 1152u, o2 = l * (191 * 8192) + c.h0 * 256;
    const u32 o3 = blockIdx.x * (l == 1 ? 18432u : l == 2 ? 24576u : l == 3 ? 8192u : 0u) + (l == 1 ? WS_P : l == 2 ? WS_M : l == 3 ? WS_X : WS_F1 + 64);
    const u32 o4 = ((2 * c.bp + (c.rank >> 1)) * 48 + l * 15) * 2304u + c.br * 1152 + c.j * 256 + (c.rank & 1) * 128;
    u32 j2;
    asm volatile("s_load_dword %0, %1, %6\n\ts_load_dword %0, %2, %7\n\ts_load_dword %0, %3, %8\n\ts_load_dword %0, %4, %9\n\ts_load_dword %0, %5, %10"
                 : "=&s"(j2) : "s"(l & 2 ? c.k : c.v), "s"(c.wo), "s"(c.wq), "s"(c.ws), "s"(c.out), "s"(o0), "s"(o1), "s"(o2), "s"(o3), "s"(o4) : "memory");
    junk = j2;
  }
  STAGE(1, 1);
  if (c.kpos == 1) { KTILE(0); WAITV(15); } else WAITV(11);
  BARRIER();
  tok = ld_async(c.ws + WS_F1 + sg * 2048, m * 128);
  STAGE(2, 2);
  if (c.kpos == 2) { KTILE(0); WAITV(15); } else WAITV(11);
  asm volatile("s_waitcnt lgkmcnt(0)" :: "s"(junk), "s"(c.junk) : "memory");
  PIN(tok); BARRIER();
  { const u32 line = (((blockIdx.x >> 3) & 7) * 4 + l) * 8 + (lane & 7);
    asm volatile("s_mov_b32 m0, %0\n\ts_nop 0\n\tglobal_load_lds_dword %1, %2" :: "s"(base + JUNK), "v"(line * 128), "s"(c.pc & ~255ull) : "memory", "m0"); }
  tok += TOKK;
  if (l == 3) {
    st_one(c.ws + WS_R + sg * 2048, m * 128 + me * 8, tok, 0xffffull);
    *(LAS u64*)(sm + MISC + m * 8) = tok; *(LAS u32*)(sm + MISC + 128) = 0u;
    asm volatile("s_waitcnt lgkmcnt(0)" ::: "memory");
  }
  int slot = 0, kt = 1;
#pragma unroll 1
  for (int st = 3; st < 9; ++st) {
    STAGE(st, slot);
    if (slot == c.kpos) { KTILE(kt); ++kt; WAITV(15); } else WAITV(11);
    slot = slot == 2 ? 0 : slot + 1;
    BARRIER();
  }
  return tok;
}

struct H2 {
  rsrc_t ro, rv;
  u32 vo, vv, ve, oso, eso, vs[3], base;
  int hown, h0, kvsh;
  template <bool EXT> DI void p2(int st) {
    const int h = h0 + ((hown + (st >> 1)) & 7);
    const u32 row = (u32)(h * 128 + (st & 1) * 64) * 1152u, vh = (u32)(h >> kvsh) * (48 * 256) + (st & 1) * 128, d = base + (st & 3) * RP;
#pragma unroll
    for (int i = 0; i < 3; ++i) dma_asm(rv, d + 16384 + i * 4096, vv, vs[i] + vh);
    dma_run<4, 16 * 1152>(ro, d, vo, oso + row);
    if (EXT) dma_asm(ro, d + 28672, ve, eso + row);
  }
};

DI void helper2(Ctx& c, LAS char* sm, int l, u64 tok) {
  const int lane = lane_id(), hkv = 32 >> c.kvsh, l4 = lane >> 4;
  H2 z;
  z.ro = mk(c.wo, 4096 * 1152); z.rv = mk(c.v, (u32)(16 * hkv) * (48 * 256));
  z.vo = l4 * 1152 + ((lane & 15) ^ ((l4 | ((l & 2) << 1)) << 1)) * 16;
  z.vv = (lane >> 3) * 256 + ((lane & 7) ^ (l4 | ((l & 1) << 2))) * 16;
  z.ve = (lane >> 2) * 1152 + ((lane & 3) ^ ((lane >> 5) << 1)) * 16;
  z.oso = c.j * 256 + l * (4 * 1152); z.eso = 1024 + (c.j & 1) * 64 + l * (16 * 1152);
  { const u32 b0 = (u32)(2 * c.bp * hkv) * (48 * 256), b1 = b0 + (u32)hkv * (48 * 256);
    z.vs[0] = b0 + l * 2048; z.vs[1] = l < 2 ? b0 + (l + 4) * 2048 : b1 + (l - 2) * 2048; z.vs[2] = b1 + (l + 2) * 2048; }
  z.base = (u32)(uintptr_t)sm + l * 1024; z.hown = 2 * c.hp; z.h0 = c.h0; z.kvsh = c.kvsh;
  const int sg = c.br * 8 + c.bp, me = c.rank * 4 + c.j;
  const char* F1 = c.ws + WS_F1 + sg * 2048;
  z.p2<true>(0); WAITV(8); BARRIER();
  z.p2<true>(1); WAITV(8); BARRIER();
  const u64 mytok = rdlane64(tok, me);
  const rsrc_t rp = mk(c.ws + WS_P, 256u * 18432u);
  const u32 p4 = 16384 + (l & 1) * 1024;
  LAS char* pl = sm + P_A + l * 1024 + lane * 16;
  LAS char* pl4 = sm + P_A + p4 + lane * 16;
  LAS char* pm = sm + 28672 + l * 1024 + lane * 16;
  LAS char* pm4 = sm + P_B2 + (l & 1) * 1024 + lane * 16;
  const int g = lane >> 4, m = lane & 15, hl = c.rank & 1;
  const bool xown = (c.j >> 1) == hl;
  const u32 cu = blockIdx.x, orow = (u32)((2 * c.bp + (c.rank >> 1)) * 48 + m) * 2304u + c.br * 1152;
  const bool need = m != me && (l < 3 ? (m & 3) == c.j : ((m ^ c.j) & 1) == 0);
  {
    u64 r = ld_async(c.ws + WS_R + sg * 2048, me * 128 + m * 8);
    z.p2<true>(2); z.p2<true>(3); WAITV(16); PIN(r);
    if (!__all(r == mytok))
      for (u32 spins = 0; spins < SPIN_CAP; ++spins) if (__all(ld_now(c.ws + WS_R + sg * 2048, me * 128 + m * 8) == mytok)) break;
  }
  BARRIER();
  z.p2<true>(4); WAITV(16); BARRIER();
  if (l == 3) st_one(F1, me * 128, mytok, 1ull);
  z.p2<true>(5); WAITV(16); BARRIER();

  z.p2<true>(6); WAITV(16); BARRIER();
#define PLOAD(pv, t, i) asm volatile("buffer_load_dwordx4 %0, %1, %2, %3 offen sc1" : "=v"(pv[i]) : "v"(lane * 16), "s"(rp), \
    "s"((u32)((c.bp * 4 + ((c.j + (t)) & 3)) * 8 + c.rank * 2 + c.br) * 18432u + ((i) < 4 ? l * 1024 + (i) * 4096 : p4)) : "memory")
#define PUTA(pv, i) do { PIN(pv[i]); if ((i) < 4 || l < 2) *(LAS u32x4*)((i) < 4 ? pl + (i) * 4096 : pl4) = pv[i]; } while (0)
#define PUTB(pv, i) do { PIN(pv[i]); if ((i) < 4 || l < 2) *(LAS u32x4*)((i) < 2 ? pm + (i) * RP : (i) < 4 ? pm + 2 * RP + ((i) - 2) * RP : pm4) = pv[i]; } while (0)
  u32x4 pa[5], pb[5], pc[5];
  {
    const u64 want = __shfl(tok, c.rank * 4 + (lane & 3));
    u64 r = ld_async(F1, (c.rank * 4 + (lane & 3)) * 128);
    z.p2<true>(7); WAITV(8); PIN(r);
    if (!__all(r == want))
      for (u32 spins = 0; spins < SPIN_CAP; ++spins) if (__all(ld_now(F1, (c.rank * 4 + (lane & 3)) * 128) == want)) break;
  }

  PLOAD(pa, 1, 0); PLOAD(pa, 1, 1); PLOAD(pa, 1, 2); PLOAD(pa, 1, 3); PLOAD(pa, 1, 4); WAITV(13); BARRIER();
  z.p2<false>(8); WAITV(7); PUTA(pa, 0); BARRIER();
  PUTA(pa, 1); PUTA(pa, 2); z.p2<false>(9); PLOAD(pb, 2, 0); PLOAD(pb, 2, 1); BARRIER();
  PUTA(pa, 3); PUTA(pa, 4); z.p2<false>(10); PLOAD(pb, 2, 2); WAITV(17); LBARRIER();
  PLOAD(pb, 2, 3); PLOAD(pb, 2, 4); z.p2<false>(11); WAITV(7);
  PUTB(pb, 0); PUTB(pb, 1); PUTB(pb, 2); PUTB(pb, 3); PUTB(pb, 4); BARRIER();
  z.p2<false>(12); PLOAD(pc, 3, 0); PLOAD(pc, 3, 1); BARRIER();
  z.p2<false>(13); PLOAD(pc, 3, 2); WAITV(17); BARRIER();
  if (l == 3) st_one(c.ws + WS_F3 + sg * 2048, me * 128 + 8, mytok, 1ull);
  z.p2<false>(14); PLOAD(pc, 3, 3); PLOAD(pc, 3, 4); WAITV(19); LBARRIER();
  z.p2<false>(15); WAITV(7);
  PUTA(pc, 0); PUTA(pc, 1); PUTA(pc, 2); PUTA(pc, 3); PUTA(pc, 4); WAITV(0); LBARRIER();
  BARRIER();
  if (l == 3 && !xown) return;
  for (u32 spins = 0; spins < SPIN_CAP; ++spins) if (__all(!need || ld_now(c.ws + WS_F3 + sg * 2048, m * 128 + (l == 3 ? 8 : 0)) == tok)) break;
  {
    const rsrc_t ro = mk(c.out, 768 * 2304);
    if (l < 3) {
      const rsrc_t rm = mk(c.ws + WS_M, 1024u * 6144u);
      u32x4 in[3][2];
#pragma unroll
      for (int r = 0; r < 3; ++r) {
        const u32 pcu = (cu & ~6u) | (((c.rank + 1 + r) & 3) << 1);
#pragma unroll
        for (int i = 0; i < 2; ++i) in[r][i] = __builtin_amdgcn_raw_buffer_load_b128(rm, lane * 16 + i * 1024, (pcu * 4 + c.rank) * 6144u + l * 2048, 16);
      }
#pragma unroll
      for (int c2 = 0; c2 < 2; ++c2) {
        f32x4 a = *(LAS f32x4*)(sm + OWN + (l * 4 + 2 * c2) * 1024 + lane * 16), d = *(LAS f32x4*)(sm + OWN + (l * 4 + 2 * c2 + 1) * 1024 + lane * 16);
#pragma unroll
        for (int r = 0; r < 3; ++r) {
          const u32x4 v = in[r][c2];
          a += (f32x4){bflo(v[0]), bfhi(v[0]), bflo(v[1]), bfhi(v[1])};
          d += (f32x4){bflo(v[2]), bfhi(v[2]), bflo(v[3]), bfhi(v[3])};
        }
        const u32x2 pa = pk4(a), pd = pk4(d);
        auto s0 = __builtin_amdgcn_permlane16_swap(pa[0], pd[0], false, false);
        auto s1 = __builtin_amdgcn_permlane16_swap(pa[1], pd[1], false, false);
        __builtin_amdgcn_raw_buffer_store_b128((u32x4){s0[0], s1[0], s0[1], s1[1]}, ro,
            orow + l * (16 * 2304) + (128 * c.j + 64 * hl + 32 * c2 + 16 * (g & 1) + 8 * (g >> 1)) * 2, 0, 0);
      }
    } else {
      const rsrc_t rxx = mk(c.ws + WS_X, 1024u * 2048u);
      u32x2 xin[7][3];
#pragma unroll
      for (int r = 0; r < 7; ++r) {
        const u32 pcu = ((cu & ~6u) | (((c.rank + 1 + r) & 3) << 1)) ^ (r >= 3 ? 16u : 0u);
#pragma unroll
        for (int n = 0; n < 3; ++n) xin[r][n] = __builtin_amdgcn_raw_buffer_load_b64(rxx, lane * 8 + n * 512, (pcu * 4 + c.rank) * 2048u, 16);
      }
#pragma unroll
      for (int n = 0; n < 3; ++n) {
        f32x4 a = *(LAS f32x4*)(sm + XL + n * 1024 + lane * 16);
#pragma unroll
        for (int r = 0; r < 7; ++r) a += (f32x4){bflo(xin[r][n][0]), bfhi(xin[r][n][0]), bflo(xin[r][n][1]), bfhi(xin[r][n][1])};
        __builtin_amdgcn_raw_buffer_store_b64(pk4(a), ro, orow + n * (16 * 2304) + (512 + 32 * (c.j & 1) + 16 * hl + 4 * g) * 2, 0, 0);
      }
    }
  }

}

template <bool ADV, int I0, int I1> DI void half1(f32x4 (&Q)[8][3], const u32x4 (&A)[8], const u32x4 (&B)[3], u32x4 (&An)[8], u32x4 (&Bn)[3],
                                  LAS char* (&w)[8], LAS char* (&x)[2], int u, int adv) {
  u32x2 lo[8];
#pragma unroll
  for (int i = I0; i < I1; ++i) {
    Q[i / 3][i % 3] = mfma(A[i / 3], B[i % 3], Q[i / 3][i % 3]);
    constexpr int what[19] = {70, 71, 00, 01, 100, 10, 11, 101, 20, 21, 102, 30, 31, 60, 40, 41, 61, 50, 51};
    if (i < 19) {
      const int k = what[i];
      if (k >= 100) Bn[k - 100] = lds128(x[u] + (k - 100) * 2048);
      else if (k % 10 == 0) lo[k / 10] = trd(w[k / 10] + u * 8192);
      else An[k / 10] = cat(lo[k / 10], trd(w[k / 10] + u * 8192 + 1024));
    } else if (ADV) {
      if (i < 23) { w[2 * (i - 19)] = vadd(w[2 * (i - 19)], adv); w[2 * (i - 19) + 1] = vadd(w[2 * (i - 19) + 1], adv); }
      else { x[0] = vadd(x[0], adv); x[1] = vadd(x[1], adv); }
    }
    SB();
  }
}
struct G2 { u32x4 v[3], w[5]; u32x2 lo; };
struct A2 { LAS char* w[4]; LAS char* e; LAS char* v[2]; };
template <bool EXT> DI void rd2(int i, G2& f, const A2& a, int off, int u) {
  if (i < 3) f.v[i] = lds128(a.v[u] + off + 16384 + i * 2048);
  else if (i < 11) { const int ct = (i - 3) >> 1; if ((i - 3) & 1) f.w[ct] = cat(f.lo, trd(a.w[ct] + off + u * 8192 + 1024)); else f.lo = trd(a.w[ct] + off + u * 8192); }
  else if (EXT && i == 11) f.lo = trd(a.e + off + 28672 + u * 2048);
  else if (EXT && i == 12) f.w[4] = cat(f.lo, trd(a.e + off + 28672 + u * 2048 + 256));
}
template <int SH> DI void move2(int i, A2& a) {
  if (SH && i == 0) { a.v[0] = vadd(a.v[0], SH); a.v[1] = vadd(a.v[1], SH); }
  if (SH && i == 1) { a.w[0] = vadd(a.w[0], SH); a.w[1] = vadd(a.w[1], SH); }
  if (SH && i == 2) { a.w[2] = vadd(a.w[2], SH); a.w[3] = vadd(a.w[3], SH); a.e = vadd(a.e, SH); }
}
template <int BAR> DI void open2() {
  if (BAR == 2) WAITV(0);
  if (BAR) LBARRIER();
}
template <bool E, bool PACK, int SH, int BAR>
DI void stage2(f32x4 (&U)[3][5], G2& f0, G2& f1, A2& a, int off, u32x4 (&UA)[5], u32x2 (&UC)[5]) {
  constexpr int N = E ? 15 : 12, R = E ? 13 : 11;
  const f32x4 z = {0.f, 0.f, 0.f, 0.f};
#pragma unroll
  for (int i = 0; i < N; ++i) {
    U[i % 3][i / 3] = mfma(f1.v[i % 3], f1.w[i / 3], U[i % 3][i / 3]);
    move2<SH>(i, a);
    if (i == 2) { SB(); open2<BAR>(); }
    if (i >= 3) rd2<E>(i - 3, f0, a, off, 0);
    SB();
  }
#pragma unroll
  for (int i = 0; i < N; ++i) {
    const int ct = i / 3, kt = i % 3;
    if (PACK) { const u32x2 t = pk4(U[kt][ct]); if (kt == 0) { UA[ct][0] = t[0]; UA[ct][1] = t[1]; } else if (kt == 1) { UA[ct][2] = t[0]; UA[ct][3] = t[1]; } else UC[ct] = t; SB(); }
    U[kt][ct] = mfma(f0.v[kt], f0.w[ct], PACK ? z : U[kt][ct]);
    const int r = N - 3 + i + (!E && i > 2 ? 1 : 0);
    if (r < R) rd2<E>(r, f0, a, off, 0); else rd2<E>(r - R, f1, a, off, 1);
    if (!E && i == 2) rd2<E>(r + 1 - R, f1, a, off, 1);
    SB();
  }
}
template <bool PE, bool CE, bool READ, bool FIRST, int BS, int SH, int BAR>
DI void finish2(f32x4 (&O)[5][3], f32x4 (&U)[3][5], const u32x4 (&A0)[5], const u32x2 (&C0)[5], G2& f0, G2& f1, A2& a, int off, LAS char* p, LAS char* pc, LAS char* pc2) {
  constexpr int N = PE ? 15 : 12, R = CE ? 13 : 11;
  u32x4 pa0[3], pa1[3], pcc[3], A1[5]; u32x2 C1[5], lo[3];
#pragma unroll
  for (int i = 0; i < N; ++i) {
    U[i % 3][i / 3] = mfma(f1.v[i % 3], f1.w[i / 3], U[i % 3][i / 3]);
    move2<SH>(i, a);
    if (i == 2) { SB(); open2<BAR>(); }
    if (i >= 3 && i < 6) pa0[i - 3] = lds128(p + (i - 3) * 1024);
    else if (i >= 6 && i < 9) pa1[i - 6] = lds128(p + BS + (i - 6) * 1024);
    else if (READ && i >= 9) rd2<CE>(i - 9, f0, a, off, 0);
    SB();
  }
#pragma unroll
  for (int i = 0; i < N; ++i) {
    const int ct = i / 3, n = i % 3;
    O[ct][n] = mfma(A0[ct], pa0[n], FIRST ? (f32x4){0.f, 0.f, 0.f, 0.f} : O[ct][n]);
    const u32x2 t = pk4(U[n][ct]);
    if (n == 0) { A1[ct][0] = t[0]; A1[ct][1] = t[1]; } else if (n == 1) { A1[ct][2] = t[0]; A1[ct][3] = t[1]; } else C1[ct] = t;
    SB();
  }
#pragma unroll
  for (int i = 0; i < N; ++i) {
    O[i / 3][i % 3] = mfma(A1[i / 3], pa1[i % 3], O[i / 3][i % 3]);
    if (i < 3) lo[i] = lds64(i < 2 ? pc + i * 512 : pc2);
    else if (i < 6) pcc[i - 3] = cat(lo[i - 3], lds64(i < 5 ? pc + BS + (i - 3) * 512 : pc2 + 512));
    else if (READ) rd2<CE>(i + N - 15, f0, a, off, 0);
    SB();
  }
#pragma unroll
  for (int i = 0; i < N; ++i) {
    O[i / 3][i % 3] = mfma(cat(C0[i / 3], C1[i / 3]), pcc[i % 3], O[i / 3][i % 3]);
    if (READ) { const int r = 2 * N - 15 + i; if (r < R) rd2<CE>(r, f0, a, off, 0); }
    SB();
  }
}
template <bool E> DI void first2(f32x4 (&U)[3][5], const G2& f0, G2& f1, const A2& a, int off) {
#pragma unroll
  for (int i = 0; i < (E ? 15 : 12); ++i) {
    U[i % 3][i / 3] = mfma(f0.v[i % 3], f0.w[i / 3], (f32x4){0.f, 0.f, 0.f, 0.f});
    rd2<E>(i, f1, a, off, 1);
    SB();
  }
}

DI void tail(Ctx& c, LAS char* sm, int wv, f32x4 (&O)[5][3]) {
  const int lane = lane_id();
  if (wv == c.rank) {
#pragma unroll
    for (int n = 0; n < 3; ++n)
#pragma unroll
      for (int ct = 0; ct < 4; ++ct) *(LAS f32x4*)(sm + OWN + (n * 4 + ct) * 1024 + lane * 16) = O[ct][n];
    LBARRIER();
    return;
  }
  const rsrc_t rm = mk(c.ws + WS_M, 1024u * 6144u);
#pragma unroll
  for (int n = 0; n < 3; ++n)
#pragma unroll
    for (int c2 = 0; c2 < 2; ++c2) __builtin_amdgcn_raw_buffer_store_b128(cat(pk4(O[2 * c2][n]), pk4(O[2 * c2 + 1][n])), rm, (blockIdx.x * 4 + wv) * 6144u + lane * 16 + (n * 2 + c2) * 1024, 0, 16);
  const int me = c.rank * 4 + c.j;
  const u64 tok = *(LAS u64*)(sm + MISC + me * 8);
  BARRIER();
  WAITV(0);

  u32 old;
  asm volatile("s_mov_b64 exec, 1\n\tds_add_rtn_u32 %0, %1, %2\n\ts_waitcnt lgkmcnt(0)\n\ts_mov_b64 exec, -1" : "=&v"(old) : "v"((u32)(uintptr_t)sm + MISC + 128), "v"(1u) : "memory");
  if (__builtin_amdgcn_readfirstlane(old) == 2u) st_one(c.ws + WS_F3 + (c.br * 8 + c.bp) * 2048, me * 128, tok, 1ull);
}

DI void compute(Ctx& c, LAS char* sm, int wv) {
  char* ws = c.ws;
  const int bl = wv >> 1, hl = wv & 1;
  const u32 cu = blockIdx.x;
  int lane = lane_id(), g = lane >> 4, m = lane & 15, q = m >> 2, p = lane & 3;
  u32 wbase = (8 * g + q) * 256 + 32 * (q | ((g & 1) << 2)) + 8 * p;
  u32 xbase = (bl * 48 + m) * 128 + ((g ^ (m >> 1)) * 16);
  u32x4 Kf[3][4];
  f32x4 Q[8][3];
  u32x4 A1[8], B1[3];
  LAS char* aw[8]; LAS char* ax[2];
#pragma unroll
  for (int T = 0; T < 8; ++T) { A1[T] = (u32x4){0, 0, 0, 0}; PIN(A1[T]);
    aw[T] = vadd(sm + (wbase ^ (T << 5)), 12288 + hl * 16384);
#pragma unroll
    for (int n = 0; n < 3; ++n) { Q[T][n] = (f32x4){0.f, 0.f, 0.f, 0.f}; PIN(Q[T][n]); } }
#pragma unroll
  for (int n = 0; n < 3; ++n) { B1[n] = (u32x4){0, 0, 0, 0}; PIN(B1[n]); }
  ax[0] = vadd(sm + xbase, 0); ax[1] = vadd(sm + (xbase ^ 64), 0);
  SB();
 asm volatile("s_waitcnt vmcnt(0) lgkmcnt(0)" :: "s"(c.junk) : "memory"); BARRIER();

#pragma unroll
  for (int kt = 0; kt < 3; ++kt) {
#pragma unroll 1
    for (int s = 0; s < 3; ++s) {
      u32x4 A0[8], B0[3];
      half1<false, 0, 20>(Q, A1, B1, A0, B0, aw, ax, 0, 0);
      if (s == c.kpos) {
        const u32 l = lane_id(), kb = KS + wv * 4096 + (l & 15) * 256 + ((l >> 5) ^ (l & 15)) * 16 + ((l >> 1) & 8);
#pragma unroll
        for (int i = 0; i < 4; ++i) Kf[kt][i] = cat(lds64(sm + (kb ^ (i << 6))), lds64(sm + (kb ^ (i << 6) ^ 32)));
      }
      half1<false, 20, 24>(Q, A1, B1, A0, B0, aw, ax, 0, 0);
      half1<true, 0, 24>(Q, A0, B0, A1, B1, aw, ax, 1, s == 2 ? -2 * QP : QP);
      LBARRIER();

    }
  }
  SB();
  lane = lane_id(); g = lane >> 4; m = lane & 15; q = m >> 2; p = lane & 3;
  wbase = (8 * g + q) * 256 + 32 * (q | ((g & 1) << 2)) + 8 * p;
  xbase = (bl * 48 + m) * 128 + ((g ^ (m >> 1)) * 16);
  A2 a;
#pragma unroll
  for (int ct = 0; ct < 4; ++ct) a.w[ct] = sm + (wbase ^ ((4 * hl + ct) << 5));
  a.e = sm + (8 * g + q) * 64 + 32 * (hl ^ (g & 1)) + 8 * p;
  a.v[0] = sm + xbase; a.v[1] = sm + (xbase ^ 64);
  G2 f0, f1;
  f32x4 O[5][3], U[3][5];
  {
    u32x4 qf[3][4];
    f32x4 S[3][3];
#pragma unroll
    for (int i = 0; i < 24; ++i) {
      Q[i & 7][i >> 3] = mfma(A1[i & 7], B1[i >> 3], Q[i & 7][i >> 3]);
      if (i >= 8) { const u32x2 t = pk4(Q[i & 7][(i >> 3) - 1]); qf[(i >> 3) - 1][(i & 7) >> 1][2 * (i & 1)] = t[0]; qf[(i >> 3) - 1][(i & 7) >> 1][2 * (i & 1) + 1] = t[1]; }
      SB();
    }

#pragma unroll
    for (int i = 0; i < 36; ++i) {
      const int n = i / 12, s = (i % 12) / 3, kt = i % 3;
      S[n][kt] = mfma(Kf[kt][s], qf[n][s], s == 0 ? (f32x4){0.f, 0.f, 0.f, 0.f} : S[n][kt]);
      if (i >= 2 && i < 10) { const u32x2 t = pk4(Q[i - 2][2]); qf[2][(i - 2) >> 1][2 * (i & 1)] = t[0]; qf[2][(i - 2) >> 1][2 * (i & 1) + 1] = t[1]; }
      if (i >= 10 && i < 23) rd2<true>(i - 10, f0, a, 0, 0);
      if (i >= 23) rd2<true>(i - 23, f1, a, 0, 1);
      SB();
    }

    const rsrc_t rp = mk(ws + WS_P, 256u * 18432u);
    const u32 pso = cu * 18432u + wv * 4096;
    LAS char* pl = vadd(sm + lane * 16, P_A + wv * 4096);
    LAS char* pl2 = vadd(sm + lane * 8, P_A + wv * 4096 + 3072);
    const int c2o = 16384 - 3072 - wv * 3584;
    float mx[3], sum[3];
    int um = 0;
#define UM() do { if (um < 15) { U[um % 3][um / 3] = mfma(f0.v[um % 3], f0.w[um / 3], (f32x4){0.f, 0.f, 0.f, 0.f}); ++um; } SB(); } while (0)
#pragma unroll
    for (int n = 0; n < 3; ++n)
      mx[n] = fmaxf(fmaxf(fmaxf(fmaxf(fmaxf(fmaxf(S[n][0][0], S[n][0][1]), S[n][0][2]), S[n][0][3]), S[n][1][0]), fmaxf(fmaxf(S[n][1][1], S[n][1][2]), S[n][1][3])),
                    fmaxf(fmaxf(fmaxf(S[n][2][0], S[n][2][1]), S[n][2][2]), S[n][2][3]));
#pragma unroll
    for (int n = 0; n < 3; ++n) { auto r = __builtin_amdgcn_permlane16_swap(__float_as_uint(mx[n]), __float_as_uint(mx[n]), false, false); mx[n] = fmaxf(__uint_as_float(r[0]), __uint_as_float(r[1])); }
#pragma unroll
    for (int n = 0; n < 3; ++n) { auto r = __builtin_amdgcn_permlane32_swap(__float_as_uint(mx[n]), __float_as_uint(mx[n]), false, false); mx[n] = fmaxf(__uint_as_float(r[0]), __uint_as_float(r[1])) * -0.12751743344863963f; }
    SB();
#pragma unroll
    for (int n = 0; n < 3; ++n)
#pragma unroll
      for (int kt = 0; kt < 3; ++kt) {
#pragma unroll
        for (int i = 0; i < 4; ++i) S[n][kt][i] = __builtin_amdgcn_exp2f(__builtin_fmaf(S[n][kt][i], 0.12751743344863963f, mx[n]));
        UM();
      }

#pragma unroll
    for (int n = 0; n < 3; ++n) { const f32x4 s4 = S[n][0] + S[n][1] + S[n][2]; sum[n] = (s4[0] + s4[1]) + (s4[2] + s4[3]); UM(); }
#pragma unroll
    for (int n = 0; n < 3; ++n) { auto r = __builtin_amdgcn_permlane16_swap(__float_as_uint(sum[n]), __float_as_uint(sum[n]), false, false); sum[n] = __uint_as_float(r[0]) + __uint_as_float(r[1]); }
#pragma unroll
    for (int n = 0; n < 3; ++n) { auto r = __builtin_amdgcn_permlane32_swap(__float_as_uint(sum[n]), __float_as_uint(sum[n]), false, false); sum[n] = __builtin_amdgcn_rcpf(__uint_as_float(r[0]) + __uint_as_float(r[1])); }
    SB();

#pragma unroll
    for (int n = 0; n < 3; ++n) {
      const u32x4 pa = cat(pk4(S[n][0] * sum[n]), pk4(S[n][1] * sum[n]));
      const u32x2 pc = pk4(S[n][2] * sum[n]);
      __builtin_amdgcn_raw_buffer_store_b128(pa, rp, pso + lane * 16 + n * 1024, 0, 16);
      __builtin_amdgcn_raw_buffer_store_b64(pc, rp, lane * 8 + 3072 + (n < 2 ? n * 512 : 0), pso + (n < 2 ? 0 : c2o), 16);
      *(LAS u32x4*)(pl + n * 1024) = pa;
      *(LAS u32x2*)(n < 2 ? pl2 + n * 512 : vadd(pl2, c2o)) = pc;
      UM();
    }
  }

  SB();
  u32x4 UA[5]; u32x2 UC[5];
  const bool xown = (wv == c.rank) && ((c.j >> 1) == (c.rank & 1));
  LAS char *pp0, *pp1, *pp2;
#define PPTR(INB) do { const int l_ = lane_id(), pb_ = (INB) ? 28672 + bl * 2 * RP : P_A + bl * 8192; \
    pp0 = vadd(sm + l_ * 16, pb_); pp1 = vadd(sm + l_ * 8, pb_ + 3072); pp2 = vadd(sm + l_ * 8, ((INB) ? P_B2 : P_A + 16384) + bl * 1024); SB(); } while (0)
#define BARK(g) ((g) > 22 ? 0 : (g) == 11 || (g) == 20 ? 2 : 1)
#define BODY(t, PE, CE) do { \
    constexpr int g0 = 9 + 4 * (t); \
    if ((t) > 0) { \
      finish2<PE, CE, true, (t) == 1, (t) == 3 ? RP : 4096, -2 * RP, BARK(g0)>(O, U, UA, UC, f0, f1, a, 0, pp0, pp1, pp2); \
      if ((t) == 2) { \
        if (xown) { _Pragma("unroll") for (int n = 0; n < 3; ++n) *(LAS f32x4*)(sm + XL + n * 1024 + lane_id() * 16) = O[4][n]; } \
        else { const rsrc_t rxx = mk(ws + WS_X, 1024u * 2048u); \
          _Pragma("unroll") for (int n = 0; n < 3; ++n) __builtin_amdgcn_raw_buffer_store_b64(pk4(O[4][n]), rxx, lane_id() * 8 + n * 512, (cu * 4 + wv) * 2048u, 16); } \
      } \
      first2<CE>(U, f0, f1, a, 0); } \
    stage2<CE, false, 0, BARK(g0 + 1)>(U, f0, f1, a, RP, UA, UC); \
    stage2<CE, true, 2 * RP, BARK(g0 + 2)>(U, f0, f1, a, 0, UA, UC); \
    stage2<CE, false, 0, BARK(g0 + 3)>(U, f0, f1, a, RP, UA, UC); PPTR((t) == 2); \
  } while (0)
  BODY(0, true, true);
  BODY(1, true, true);
  BODY(2, true, false);
  BODY(3, false, false);
  finish2<false, false, false, false, 4096, 0, 0>(O, U, UA, UC, f0, f1, a, 0, pp0, pp1, pp2);

  tail(c, sm, wv, O);
}

__global__ __launch_bounds__(512, 2) void fused_attn_block(const char* xA, const char* kA, const char* vA, const char* wqA, const char* woA,
                                                          const char* xB, const char* kB, const char* vB, const char* wqB, const char* woB,
                                                          char* out, char* ws) {
  __shared__ __attribute__((aligned(256))) char smem[LDS_BYTES];
  LAS char* sm = (LAS char*)smem;
  const int wv = __builtin_amdgcn_readfirstlane(threadIdx.x >> 6);
  Ctx c;
  const int id = blockIdx.x;
  c.br = id & 1; c.rank = (id >> 1) & 3; c.j = (id >> 3) & 3; c.bp = id >> 5;
  c.h0 = c.rank * 8; c.kvsh = 1 - c.br;
  c.hp = (c.j + c.bp) & 3;
  { const int g3 = (c.bp + c.hp) % 3;
    c.ord = g3 == 0 ? 0x875462130ull : g3 == 1 ? 0x865732401ull : 0x768435102ull;
    c.kpos = g3 == 1 ? 2 : 1; }
  {
    const u32 lane = lane_id(), l4 = lane >> 4, k0 = (u32)c.ord & 15u;
    u32 vx = (lane >> 3) * 1152 + ((lane & 7) ^ (l4 | ((wv & 1) << 2))) * 16;
    u32 vq = l4 * 8192 + ((lane & 15) ^ ((l4 | ((wv & 2) << 1)) << 1)) * 16;
    u32 xs = (u32)(c.bp * 96 + wv * 8) * 1152u + k0 * 128, qs = (u32)(c.h0 + 2 * c.hp) * 256u + wv * (4 * 8192) + k0 * (64 * 8192);
    u32 d = (u32)(uintptr_t)sm + wv * 1024;
    u32 d5 = wv < 4 ? d + 8192 : (u32)(uintptr_t)sm + JUNK, v5 = vx + (wv < 4 ? 64 * 1152 : 0x7f000000u);
    asm volatile("s_getpc_b64 %0" : "=s"(c.pc));
    asm volatile("" : "+v"(vx), "+v"(vq), "+s"(xs), "+s"(qs), "+s"(d), "+s"(d5), "+v"(v5));
    SB();
    c.x = c.br ? xB : xA; c.k = c.br ? kB : kA; c.v = c.br ? vB : vA; c.wq = c.br ? wqB : wqA; c.wo = c.br ? woB : woA;
    c.out = out; c.ws = ws;
    u32 junk = 0;
    asm volatile("s_load_dword %0, %1, %2" : "+s"(junk) : "s"(c.wq), "s"(qs) : "memory");
    const rsrc_t rx = mk(c.x, 768 * 1152), rq = mk(c.wq, 576 * 8192);
    dma_asm(rx, d, vx, xs);
#pragma unroll
    for (int i = 0; i < 4; ++i) dma_asm(rq, d + 12288 + i * 8192, vq, qs + (i & 1) * (32 * 8192) + (i >> 1) * 256);
    dma_asm(rx, d5, v5, xs);
    c.junk = junk;

  }
  u64 tok = 0;
  { int w2 = wv; asm volatile("" : "+s"(w2));
    if (__builtin_expect(w2 >= 4, 1)) tok = helper1(c, sm, w2 - 4); }
  if (__builtin_expect(wv < 4, 1)) compute(c, sm, wv);
  else helper2(c, sm, wv - 4, tok);
}

extern "C" void kernel_launch(void* const* d_in, const int* in_sizes, int n_in, void* d_out, int out_size, void* d_ws, size_t ws_size,
                              hipStream_t stream) {
  const char* const* in = (const char* const*)d_in;
  fused_attn_block<<<256, 512, 0, stream>>>(in[0], in[1], in[2], in[3], in[4], in[5], in[6], in[7], in[8], in[9], (char*)d_out, (char*)d_ws);
}
```
